```python
import torch

def _softmax(x, axis=-1):
    return torch.softmax(x, dim=axis)
def _gelu(x):
    return torch.nn.functional.gelu(x, approximate="tanh")
def _silu(x):
    return torch.nn.functional.silu(x)
def _sigmoid(x):
    return torch.sigmoid(x)
def _layernorm(x, axis=-1, eps=1e-5):
    m = x.mean(dim=axis, keepdim=True)
    v = x.var(dim=axis, keepdim=True, unbiased=False)
    return (x - m) / torch.sqrt(v + eps)
def _scatter_add(tgt, idx, src, axis=0):
    return tgt.clone().index_add_(axis, idx, src)
def _segment_sum(src, seg, n):
    out = torch.zeros((n,) + src.shape[1:], dtype=src.dtype)
    return out.index_add_(0, seg, src)
def _pad(x, pw):
    flat = tuple(v for pair in reversed(pw) for v in pair)
    return torch.nn.functional.pad(x, flat)

kernel_name = 'concat_heads__gqa_block_cached__mha_block_cached__near_ridge'

def setup_inputs():

    _w = (lambda a: (lambda t: t.to(torch.bfloat16) if t.is_floating_point() else t)(torch.as_tensor(a)))
    def _A_setup():
        B, Sq, D = 16, 32, 576
        Hq, Hkv, Sk, Dh = 32, 16, 32, 128
        assert Hq % Hkv == 0, (Hq, Hkv)
        return {
            "x": _w((lambda *s: torch.randn(*s, dtype=torch.bfloat16) * 0.8)(B, Sq, D)),
            "k_cache": _w((lambda *s: torch.randn(*s, dtype=torch.bfloat16) * 0.8)(B, Hkv, Sk, Dh)),
            "v_cache": _w((lambda *s: torch.randn(*s, dtype=torch.bfloat16) * 0.05)(B, Hkv, Sk, Dh)),
            "wq": _w((lambda *s: torch.randn(*s, dtype=torch.bfloat16) * 0.8)(D, Hq * Dh) * (D ** -0.5)),
            "wo": _w((lambda *s: torch.randn(*s, dtype=torch.bfloat16) * 0.05)(Hq * Dh, D) * ((Hq * Dh) ** -0.5)),
        }
    _A_in = _A_setup()

    def _B_setup():
        B, Sq, D = 16, 32, 576
        H, Sk, Dh = 32, 32, 128
        return {
            "x": _w((lambda *s: torch.randn(*s, dtype=torch.bfloat16) * 0.8)(B, Sq, D)),
            "k_cache": _w((lambda *s: torch.randn(*s, dtype=torch.bfloat16) * 0.8)(B, H, Sk, Dh)),
            "v_cache": _w((lambda *s: torch.randn(*s, dtype=torch.bfloat16) * 0.05)(B, H, Sk, Dh)),
            "wq": _w((lambda *s: torch.randn(*s, dtype=torch.bfloat16) * 0.8)(D, H * Dh) * (D ** -0.5)),
            "wo": _w((lambda *s: torch.randn(*s, dtype=torch.bfloat16) * 0.05)(H * Dh, D) * ((H * Dh) ** -0.5)),
        }
    _B_in = _B_setup()

    return {
        "A_x": _A_in["x"],
        "A_k_cache": _A_in["k_cache"],
        "A_v_cache": _A_in["v_cache"],
        "A_wq": _A_in["wq"],
        "A_wo": _A_in["wo"],
        "B_x": _B_in["x"],
        "B_k_cache": _B_in["k_cache"],
        "B_v_cache": _B_in["v_cache"],
        "B_wq": _B_in["wq"],
        "B_wo": _B_in["wo"],
    }

def reference(A_x, A_k_cache, A_v_cache, A_wq, A_wo, B_x, B_k_cache, B_v_cache, B_wq, B_wo):
    def _A_ref(x, k_cache, v_cache, wq, wo):
        B, Sq, D = x.shape
        Hq, Dh = 32, 128
        Hkv = k_cache.shape[1]
        rep = Hq // Hkv
        q = (lambda x, *d: x.permute(*d))((x @ wq).reshape(B, Sq, Hq, Dh), 0, 2, 1, 3)
        k = (lambda x, n, axis: torch.repeat_interleave(x, n, dim=axis))(k_cache, rep, axis=1)
        v = (lambda x, n, axis: torch.repeat_interleave(x, n, dim=axis))(v_cache, rep, axis=1)
        scores = torch.einsum("bhqd,bhkd->bhqk", q, k) * (Dh ** -0.5)
        p = _softmax(scores, axis=-1)
        o = torch.einsum("bhqk,bhkd->bhqd", p, v)
        return (lambda x, *d: x.permute(*d))(o, 0, 2, 1, 3).reshape(B, Sq, Hq * Dh) @ wo

    def _B_ref(x, k_cache, v_cache, wq, wo):
        B, Sq, D = x.shape
        H, Dh = 32, 128
        q = (lambda x, *d: x.permute(*d))((x @ wq).reshape(B, Sq, H, Dh), 0, 2, 1, 3)
        scores = torch.einsum("bhqd,bhkd->bhqk", q, k_cache) * (Dh ** -0.5)
        p = _softmax(scores, axis=-1)
        o = torch.einsum("bhqk,bhkd->bhqd", p, v_cache)
        return (lambda x, *d: x.permute(*d))(o, 0, 2, 1, 3).reshape(B, Sq, H * Dh) @ wo

    a = _A_ref(A_x, A_k_cache, A_v_cache, A_wq, A_wo)
    b = _B_ref(B_x, B_k_cache, B_v_cache, B_wq, B_wo)
    return torch.concatenate([a, b], axis=-1)
```

```cpp
#include <hip/hip_runtime.h>
#include <hip/hip_bf16.h>
typedef unsigned short u16;
__device__ __forceinline__ float bf2f(u16 v){ return __uint_as_float(((unsigned)v)<<16); }
__device__ __forceinline__ u16 f2bf(float x){ unsigned u=__float_as_uint(x); u += 0x7fffu + ((u>>16)&1u); return (u16)(u>>16); }

__global__ void k1(const u16* xA,const u16* kA,const u16* vA,const u16* wqA,
                   const u16* xB,const u16* kB,const u16* vB,const u16* wqB,u16* o){
  int id=blockIdx.x; int br=id/512, b=(id/32)%16, h=id%32;
  const u16* x=(br?xB:xA)+b*32*576; const u16* wq=(br?wqB:wqA);
  int hk= br? h : h/2; int nk= br?32:16;
  const u16* k=(br?kB:kA)+((size_t)b*nk+hk)*32*128; const u16* v=(br?vB:vA)+((size_t)b*nk+hk)*32*128;
  __shared__ float q[32][129]; __shared__ float p[32][33];
  int t=threadIdx.x;
  for(int r=0;r<32;r++){ float a=0; for(int kk=0;kk<576;kk++) a+=bf2f(x[r*576+kk])*bf2f(wq[(size_t)kk*4096+h*128+t]); q[r][t]=bf2f(f2bf(a)); }
  __syncthreads();
  for(int i=t;i<1024;i+=128){ int r=i/32,c=i%32; float a=0; for(int d=0;d<128;d++) a+=q[r][d]*bf2f(k[c*128+d]); p[r][c]=a*0.08838834764831845f; }
  __syncthreads();
  if(t<32){ float m=-1e30f; for(int c=0;c<32;c++) m=fmaxf(m,p[t][c]); float s=0; for(int c=0;c<32;c++){ float e=__expf(p[t][c]-m); p[t][c]=e; s+=e;} for(int c=0;c<32;c++) p[t][c]=bf2f(f2bf(p[t][c]/s)); }
  __syncthreads();
  for(int r=0;r<32;r++){ float a=0; for(int c=0;c<32;c++) a+=p[r][c]*bf2f(v[c*128+t]); o[((size_t)br*512+b*32+r)*4096+h*128+t]=f2bf(a); }
}
__global__ void k2(const u16* o,const u16* woA,const u16* woB,u16* out){
  int row=blockIdx.x; int br=blockIdx.y; const u16* wo=br?woB:woA; const u16* orow=o+((size_t)br*512+row)*4096;
  for(int n=threadIdx.x;n<576;n+=blockDim.x){ float a=0; for(int kk=0;kk<4096;kk++) a+=bf2f(orow[kk])*bf2f(wo[(size_t)kk*576+n]); out[(size_t)row*1152+br*576+n]=f2bf(a);}
}
extern "C" void kernel_launch(void* const* d_in,const int* in_sizes,int n_in,void* d_out,int out_size,void* d_ws,size_t ws_size,hipStream_t stream){
  u16* o=(u16*)d_ws;
  k1<<<1024,128,0,stream>>>((const u16*)d_in[0],(const u16*)d_in[1],(const u16*)d_in[2],(const u16*)d_in[3],(const u16*)d_in[5],(const u16*)d_in[6],(const u16*)d_in[7],(const u16*)d_in[8],o);
  k2<<<dim3(512,2),192,0,stream>>>(o,(const u16*)d_in[4],(const u16*)d_in[9],(u16*)d_out);
}
```

```cpp
#include <hip/hip_runtime.h>
#include <hip/hip_bf16.h>
typedef unsigned short u16;
typedef int v4i __attribute__((ext_vector_type(4)));
__device__ __forceinline__ float bf2f(u16 v){ return __uint_as_float(((unsigned)v)<<16); }
__device__ __forceinline__ u16 f2bf(float x){ unsigned u=__float_as_uint(x); u += 0x7fffu + ((u>>16)&1u); return (u16)(u>>16); }
__global__ void k1(const u16* xA,const u16* kA,const u16* vA,const u16* wqA,
                   const u16* xB,const u16* kB,const u16* vB,const u16* wqB,u16* o){
  int id=blockIdx.x; int br=id/512, b=(id/32)%16, h=id%32;
  const u16* x=(br?xB:xA)+b*32*576; const u16* wq=(br?wqB:wqA);
  int hk= br? h : h/2; int nk= br?32:16;
  const u16* k=(br?kB:kA)+((size_t)b*nk+hk)*32*128; const u16* v=(br?vB:vA)+((size_t)b*nk+hk)*32*128;
  __shared__ float q[32][129]; __shared__ float p[32][33];
  int t=threadIdx.x;
  for(int r=0;r<32;r++){ float a=0; for(int kk=0;kk<576;kk++) a+=bf2f(x[r*576+kk])*bf2f(wq[(size_t)kk*4096+h*128+t]); q[r][t]=bf2f(f2bf(a)); }
  __syncthreads();
  for(int i=t;i<1024;i+=128){ int r=i/32,c=i%32; float a=0; for(int d=0;d<128;d++) a+=q[r][d]*bf2f(k[c*128+d]); p[r][c]=a*0.08838834764831845f; }
  __syncthreads();
  if(t<32){ float m=-1e30f; for(int c=0;c<32;c++) m=fmaxf(m,p[t][c]); float s=0; for(int c=0;c<32;c++){ float e=__expf(p[t][c]-m); p[t][c]=e; s+=e;} for(int c=0;c<32;c++) p[t][c]=bf2f(f2bf(p[t][c]/s)); }
  __syncthreads();
  for(int r=0;r<32;r++){ float a=0; for(int c=0;c<32;c++) a+=p[r][c]*bf2f(v[c*128+t]); o[((size_t)br*512+b*32+r)*4096+h*128+t]=f2bf(a); }
}
__global__ void k2(const u16* o,const u16* woA,const u16* woB,u16* out){
  int row=blockIdx.x; int br=blockIdx.y; const u16* wo=br?woB:woA; const u16* orow=o+((size_t)br*512+row)*4096;
  for(int n=threadIdx.x;n<576;n+=192){ float a=0; for(int kk=0;kk<4096;kk++) a+=bf2f(orow[kk])*bf2f(wo[(size_t)kk*576+n]); out[(size_t)row*1152+br*576+n]=f2bf(a);}
}
__device__ __forceinline__ v4i mk_rsrc(const void* p, unsigned bytes) {
  unsigned long long a = (unsigned long long)p;
  v4i r; r.x = __builtin_amdgcn_readfirstlane((int)(unsigned)a); r.y = __builtin_amdgcn_readfirstlane((int)(unsigned)(a >> 32)); r.z = (int)bytes; r.w = 0x00020000; return r;
}
__device__ __forceinline__ void dma(v4i rs, unsigned voff, unsigned soff, unsigned ldsaddr) {
  asm volatile("s_mov_b32 m0, %3\n\ts_nop 0\n\tbuffer_load_dwordx4 %0, %1, %2 offen POLICY lds" :: "v"(voff), "s"(rs), "s"(soff), "s"(ldsaddr) : "memory");
}
__global__ void __launch_bounds__(512,2) probe(const char* kA,const char* vA,const char* wqA,const char* woA,const char* kB,const char* vB,const char* wqB,const char* woB, unsigned long long* rec){
  extern __shared__ __attribute__((aligned(16))) char lds[];
  unsigned long long t0, t1, t2;
  asm volatile("s_memrealtime %0\n\ts_waitcnt lgkmcnt(0)" : "=s"(t0) :: "memory");
  unsigned long long p = (unsigned long long)wqA;
  asm volatile("s_waitcnt lgkmcnt(0)\n\ts_memrealtime %0\n\ts_waitcnt lgkmcnt(0)" : "=s"(t1) : "s"(p) : "memory");
  unsigned v = *(const volatile unsigned*)(wqA + blockIdx.x * 18432 + threadIdx.x * 4);
  asm volatile("s_waitcnt vmcnt(0)\n\ts_memrealtime %0\n\ts_waitcnt lgkmcnt(0)" : "=s"(t2) : "v"(v) : "memory");
  if(threadIdx.x==0){ rec[blockIdx.x*4]=t0; rec[blockIdx.x*4+1]=t1; rec[blockIdx.x*4+2]=t2; }
}
__global__ void spin(const unsigned long long* rec){
  unsigned long long mn=~0ull,mx=0,sum1=0,sum2=0,mx2=0; for(int i=0;i<256;i++){ mn=min(mn,rec[4*i]); mx=max(mx,rec[4*i]); sum1+=rec[4*i+1]-rec[4*i]; sum2+=rec[4*i+2]-rec[4*i+1]; mx2=max(mx2,rec[4*i+2]); }
  unsigned long long d= 0==0 ? (mx-mn)*1000ull : 0==1 ? sum1*1000ull/256 : 0==2 ? sum2*1000ull/256 : (mx2-mn)*1000ull; if(d>3000000ull) d=3000000ull;
  unsigned long long s=__builtin_amdgcn_s_memrealtime();
  while(__builtin_amdgcn_s_memrealtime()-s<d) __builtin_amdgcn_s_sleep(8);
}
extern "C" void kernel_launch(void* const* d_in,const int* in_sizes,int n_in,void* d_out,int out_size,void* d_ws,size_t ws_size,hipStream_t stream){
  u16* o=(u16*)d_ws; unsigned long long* rec=(unsigned long long*)((char*)d_ws+(64<<20));
  hipFuncSetAttribute((const void*)probe, hipFuncAttributeMaxDynamicSharedMemorySize, 150*1024);
  probe<<<256,512,150*1024,stream>>>((const char*)d_in[1],(const char*)d_in[2],(const char*)d_in[3],(const char*)d_in[4],(const char*)d_in[6],(const char*)d_in[7],(const char*)d_in[8],(const char*)d_in[9],rec);
  k1<<<1024,128,0,stream>>>((const u16*)d_in[0],(const u16*)d_in[1],(const u16*)d_in[2],(const u16*)d_in[3],(const u16*)d_in[5],(const u16*)d_in[6],(const u16*)d_in[7],(const u16*)d_in[8],o);
  k2<<<dim3(512,2),192,0,stream>>>(o,(const u16*)d_in[4],(const u16*)d_in[9],(u16*)d_out);
  spin<<<1,1,0,stream>>>(rec);
}
```

```cpp
#include <hip/hip_runtime.h>

constexpr int LDS_BYTES = 147472;

__global__ void __launch_bounds__(512, 2)
fused_attn(const void* xA, const void* kA, const void* vA, const void* wqA, const void* woA,
           const void* xB, const void* kB, const void* vB, const void* wqB, const void* woB, void* out, void* ws) {
  asm volatile(
      "s_and_b32 s5, %1, 1\n\t"
      "s_mul_i32 s14, s5, 40\n\t"
      "v_lshrrev_b32 v2, 6, %2\n\t"
      "s_load_dwordx8 s[20:27], %0, s14 offset:0x0\n\t"
      "s_load_dwordx2 s[18:19], %0, s14 offset:0x20\n\t"
      "s_load_dwordx4 s[28:31], %0, 0x50\n\t"
      "s_getpc_b64 s[60:61]\n\t"
      "LPC0%=:\n\t"
      "s_bfe_u32 s6, %1, 0x20003\n\t"
      "v_lshrrev_b32 v3, 4, %2\n\t"
      "v_lshl_add_u32 v3, v3, 2, s6\n\t"
      "v_lshlrev_b32 v3, 7, v3\n\t"
      "v_min_u32 v3, LEND%=-LPC0%=, v3\n\t"
      "s_mov_b32 exec_lo, 0x10001\n\t"
      "s_mov_b32 exec_hi, 0x10001\n\t"
      "global_load_dword v255, v3, s[60:61]\n\t"
      "s_mov_b64 exec, -1\n\t"
      "v_readfirstlane_b32 s4, v2\n\t"
      "v_and_b32 v1, 63, %2\n\t"
      "s_bfe_u32 s6, %1, 0x20003\n\t"
      "s_lshr_b32 s7, %1, 5\n\t"
      "s_bfe_u32 s8, %1, 0x20001\n\t"
      "s_lshl3_add_u32 s8, s8, s7\n\t"
      "s_bitcmp1_b32 s7, 0\n\t"
      "s_cselect_b32 s12, 3, 6\n\t"
      "s_cselect_b32 s13, 4, 4\n\t"
      "s_cselect_b32 s14, 7, 8\n\t"
      "s_cselect_b32 s15, 4, 4\n\t"
      "s_cmp_eq_u32 s5, 0\n\t"
      "s_cselect_b32 s12, s14, s12\n\t"
      "s_cselect_b32 s13, s15, s13\n\t"
      "s_bitcmp1_b32 s7, 0\n\t"
      "s_movk_i32 s17, 216\n\t"
      "s_cselect_b32 s17, 180, s17\n\t"
      "s_cmp_lt_u32 s4, 4\n\t"
      "s_cbranch_scc1 LCOMP%=\n\t"
      "s_setprio 0\n\t"
      "s_sub_u32 s61, s4, 4\n\t"
      "v_lshrrev_b32 v10, 4, v1\n\t"
      "v_lshrrev_b32 v13, 3, v1\n\t"
      "v_and_b32 v14, 7, v1\n\t"
      "v_and_b32 v12, 1, v10\n\t"
      "v_lshlrev_b32 v12, 1, v12\n\t"
      "v_xor_b32 v12, v14, v12\n\t"
      "v_lshlrev_b32 v12, 4, v12\n\t"
      "v_lshl_or_b32 v2, v13, 13, v12\n\t"
      "v_xor_b32 v3, 64, v2\n\t"
      "v_add_u32 v3, 64512, v3\n\t"
      "v_add_u32 v4, 128, v2\n\t"
      "v_add_u32 v5, 128, v3\n\t"
      "v_xor_b32 v14, v14, v10\n\t"
      "v_lshlrev_b32 v14, 4, v14\n\t"
      "v_mul_u32_u24 v13, 0x480, v13\n\t"
      "v_add_u32 v6, v13, v14\n\t"
      "v_xor_b32 v7, 64, v6\n\t"
      "v_add_u32 v7, 0x2000, v7\n\t"
      "v_add_u32 v8, 0x4000, v6\n\t"
      "v_add_u32 v9, 0x4000, v7\n\t"
      "s_add_u32 s50, s6, s7\n\t"
      "s_and_b32 s50, s50, 3\n\t"
      "s_mul_i32 s62, s50, 0x11111111\n\t"
      "s_mov_b32 s14, 0x32132010\n\t"
      "s_mov_b32 s15, 0x44400400\n\t"
      "s_bitcmp1_b32 s7, 0\n\t"
      "s_cselect_b32 s14, 0x32103210, s14\n\t"
      "s_cselect_b32 s15, 0x44440000, s15\n\t"
      "s_cselect_b32 s34, 4, 2\n\t"
      "s_add_u32 s62, s62, s14\n\t"
      "s_and_b32 s62, s62, 0x33333333\n\t"
      "s_or_b32 s62, s62, s15\n\t"
      "s_lshl_b32 s54, s61, 12\n\t"
      "s_lshl_b32 s14, s8, 8\n\t"
      "s_lshl_b32 s15, s61, 17\n\t"
      "s_add_u32 s14, s14, s15\n\t"
      "s_lshl2_add_u32 s15, s6, s61\n\t"
      "s_mul_i32 s16, s15, 36864\n\t"
      "s_mov_b32 s70, -1\n\t"
      "s_mov_b32 s71, 0x20000\n\t"
      "s_mov_b32 s66, -1\n\t"
      "s_mov_b32 s67, 0x20000\n\t"
      "s_mov_b32 s48, 0\n\t"
      "s_mov_b32 s49, 0\n\t"
      "s_mov_b32 s52, 0\n\t"
      "s_mov_b32 s51, s12\n\t"
      "s_mov_b32 s63, 0\n\t"
      "s_mov_b32 s35, -1\n\t"
      "s_waitcnt lgkmcnt(0)\n\t"
      "s_add_u32 s68, s26, s14\n\t"
      "s_addc_u32 s69, s27, 0\n\t"
      "s_add_u32 s64, s20, s16\n\t"
      "s_addc_u32 s65, s21, 0\n\t"
      "LLQ%=:\n\t"
      "s_cmp_lt_u32 s48, 2\n\t"
      "s_cbranch_scc1 LP1%=\n\t"
      "s_cmp_eq_u32 s48, s35\n\t"
      "s_cbranch_scc1 LW91%=\n\t"
      "s_waitcnt vmcnt(8)\n\t"
      "LW91%=:\n\t"
      "s_waitcnt vmcnt(9)\n\t"
      "s_barrier\n\t"
      "LP1%=:\n\t"
      "s_add_u32 s48, s48, 1\n\t"
      "s_add_u32 m0, s49, s54\n\t"
      "s_lshl_b32 s14, s50, 19\n\t"
      "buffer_load_dwordx4 v2, s[68:71], s14 offen lds\n\t"
      "buffer_load_dwordx4 v3, s[68:71], s14 offen offset:1024 lds\n\t"
      "s_add_u32 m0, m0, 2048\n\t"
      "s_nop 0\n\t"
      "buffer_load_dwordx4 v4, s[68:71], s14 offen lds\n\t"
      "buffer_load_dwordx4 v5, s[68:71], s14 offen offset:1024 lds\n\t"
      "s_add_u32 m0, m0, 14336\n\t"
      "s_lshl_b32 s14, s50, 7\n\t"
      "buffer_load_dwordx4 v6, s[64:67], s14 offen lds\n\t"
      "buffer_load_dwordx4 v7, s[64:67], s14 offen offset:1024 lds\n\t"
      "buffer_load_dwordx4 v8, s[64:67], s14 offen offset:2048 lds\n\t"
      "buffer_load_dwordx4 v9, s[64:67], s14 offen offset:3072 lds\n\t"
      "s_cmp_lg_u32 s48, 1\n\t"
      "s_cbranch_scc1 LNI%=\n\t"
      "s_barrier\n\t"
      "LNI%=:\n\t"
      "s_add_u32 s49, s49, 36864\n\t"
      "s_cmp_eq_u32 s49, 147456\n\t"
      "s_cselect_b32 s49, 0, s49\n\t"
      "s_lshr_b32 s62, s62, 4\n\t"
      "s_or_b32 s62, s62, 0x80000000\n\t"
      "s_and_b32 s50, s62, 15\n\t"
      "s_cmp_lg_u32 s48, 2\n\t"
      "s_cbranch_scc1 LNSU%=\n\t"
      "s_lshr_b32 s9, s8, 2\n\t"
      "s_and_b32 s10, s8, 3\n\t"
      "s_add_u32 s11, s10, s6\n\t"
      "s_and_b32 s11, s11, 3\n\t"
      "s_and_b32 s57, s8, 28\n\t"
      "s_xor_b32 s58, s5, 1\n\t"
      "s_mov_b32 s53, s10\n\t"
      "s_lshr_b32 s17, s17, 2\n\t"
      "s_lshl_b32 s55, s61, 13\n\t"
      "s_mul_i32 s56, s61, 9216\n\t"
      "s_lshr_b32 s59, s8, s58\n\t"
      "s_lshl_b32 s59, s59, 13\n\t"
      "s_lshr_b32 s14, s13, 1\n\t"
      "s_bcnt1_i32_b32 s14, s14\n\t"
      "s_sub_u32 s60, 13, s14\n\t"
      "s_lshl2_add_u32 s14, s6, s61\n\t"
      "s_add_u32 s15, s5, 17\n\t"
      "s_lshl_b32 s14, s14, s15\n\t"
      "s_add_u32 s36, s22, s14\n\t"
      "s_addc_u32 s37, s23, 0\n\t"
      "s_add_u32 s40, s24, s14\n\t"
      "s_addc_u32 s41, s25, 0\n\t"
      "s_lshl_b32 s14, s9, 4\n\t"
      "s_add_u32 s14, s14, s61\n\t"
      "s_mul_i32 s14, s14, 36864\n\t"
      "s_mul_i32 s15, s11, 288\n\t"
      "s_add_u32 s14, s14, s15\n\t"
      "s_add_u32 s44, s18, s14\n\t"
      "s_addc_u32 s45, s19, 0\n\t"
      "s_mov_b32 s38, -1\n\t"
      "s_mov_b32 s39, 0x20000\n\t"
      "s_mov_b32 s42, -1\n\t"
      "s_mov_b32 s43, 0x20000\n\t"
      "s_mov_b32 s46, -1\n\t"
      "s_mov_b32 s47, 0x20000\n\t"
      "v_and_b32 v11, 15, v1\n\t"
      "v_xor_b32 v11, v11, v10\n\t"
      "v_lshlrev_b32 v11, 4, v11\n\t"
      "v_lshl_or_b32 v10, v10, 8, v11\n\t"
      "v_xor_b32 v11, 64, v10\n\t"
      "v_xor_b32 v12, 0x80, v10\n\t"
      "v_xor_b32 v13, 0xc0, v10\n\t"
      "s_movk_i32 s14, 3641\n\t"
      "s_movk_i32 s15, 864\n\t"
      "v_lshlrev_b32 v24, 4, v1\n\t"
      "v_add_u32 v23, 0, v1\n\t"
      "v_mul_u32_u24 v23, s14, v23\n\t"
      "v_lshrrev_b32 v23, 16, v23\n\t"
      "v_mad_u32_u24 v14, v23, s15, v24\n\t"
      "v_add_u32 v23, 64, v1\n\t"
      "v_mul_u32_u24 v23, s14, v23\n\t"
      "v_lshrrev_b32 v23, 16, v23\n\t"
      "v_mad_u32_u24 v15, v23, s15, v24\n\t"
      "v_add_u32 v23, 128, v1\n\t"
      "v_mul_u32_u24 v23, s14, v23\n\t"
      "v_lshrrev_b32 v23, 16, v23\n\t"
      "v_mad_u32_u24 v16, v23, s15, v24\n\t"
      "v_add_u32 v23, 192, v1\n\t"
      "v_mul_u32_u24 v23, s14, v23\n\t"
      "v_lshrrev_b32 v23, 16, v23\n\t"
      "v_mad_u32_u24 v17, v23, s15, v24\n\t"
      "v_add_u32 v17, 3072, v17\n\t"
      "v_add_u32 v23, 256, v1\n\t"
      "v_mul_u32_u24 v23, s14, v23\n\t"
      "v_lshrrev_b32 v23, 16, v23\n\t"
      "v_mad_u32_u24 v18, v23, s15, v24\n\t"
      "v_add_u32 v18, 3072, v18\n\t"
      "v_add_u32 v23, 320, v1\n\t"
      "v_mul_u32_u24 v23, s14, v23\n\t"
      "v_lshrrev_b32 v23, 16, v23\n\t"
      "v_mad_u32_u24 v19, v23, s15, v24\n\t"
      "v_add_u32 v19, 3072, v19\n\t"
      "v_add_u32 v23, 384, v1\n\t"
      "v_mul_u32_u24 v23, s14, v23\n\t"
      "v_lshrrev_b32 v23, 16, v23\n\t"
      "v_mad_u32_u24 v20, v23, s15, v24\n\t"
      "v_add_u32 v20, 6144, v20\n\t"
      "v_add_u32 v23, 448, v1\n\t"
      "v_mul_u32_u24 v23, s14, v23\n\t"
      "v_lshrrev_b32 v23, 16, v23\n\t"
      "v_mad_u32_u24 v21, v23, s15, v24\n\t"
      "v_add_u32 v21, 6144, v21\n\t"
      "v_add_u32 v23, 512, v1\n\t"
      "v_mul_u32_u24 v23, s14, v23\n\t"
      "v_lshrrev_b32 v23, 16, v23\n\t"
      "v_mad_u32_u24 v22, v23, s15, v24\n\t"
      "v_add_u32 v22, 6144, v22\n\t"
      "LNSU%=:\n\t"
      "s_cmp_lg_u32 s63, s34\n\t"
      "s_cbranch_scc1 LNT8%=\n\t"
      "s_cmp_gt_u32 s61, 1\n\t"
      "s_cbranch_scc1 LT8X%=\n\t"
      "v_lshrrev_b32 v25, 1, v1\n\t"
      "v_and_b32 v26, 1, v1\n\t"
      "v_lshlrev_b32 v26, 7, v26\n\t"
      "v_lshl_or_b32 v25, v25, 13, v26\n\t"
      "s_lshl1_add_u32 s14, s6, s61\n\t"
      "s_add_u32 s14, s14, 64\n\t"
      "s_lshl_b32 s14, s14, 16\n\t"
      "s_lshl_b32 s15, s8, 8\n\t"
      "s_add_u32 s14, s14, s15\n\t"
      "v_add_u32 v25, s14, v25\n\t"
      "s_mov_b64 exec, 0xffff\n\t"
      "global_load_dword v27, v25, s[26:27]\n\t"
      "s_branch LT8E%=\n\t"
      "LT8X%=:\n\t"
      "s_lshl3_add_u32 s14, s6, s7\n\t"
      "s_lshl1_add_u32 s14, s14, s61\n\t"
      "s_sub_u32 s14, s14, 2\n\t"
      "s_mul_i32 s14, s14, 9216\n\t"
      "s_add_u32 s14, s14, 1024\n\t"
      "v_mul_u32_u24 v25, 0x480, v1\n\t"
      "v_add_u32 v25, s14, v25\n\t"
      "s_mov_b64 exec, 0xff\n\t"
      "global_load_dword v27, v25, s[20:21]\n\t"
      "LT8E%=:\n\t"
      "s_mov_b64 exec, -1\n\t"
      "s_mov_b32 s35, s48\n\t"
      "LNT8%=:\n\t"
      "s_add_u32 s63, s63, 1\n\t"
      "s_sub_u32 s51, s51, 1\n\t"
      "s_cmp_lg_u32 s51, 0\n\t"
      "s_cbranch_scc1 LLQ%=\n\t"
      "LLI%=:\n\t"
      "s_and_b32 s14, s13, 1\n\t"
      "s_cmp_lg_u32 s14, s52\n\t"
      "s_cbranch_scc1 LLNK%=\n\t"
      "s_cmp_eq_u32 s48, s35\n\t"
      "s_cbranch_scc1 LW92%=\n\t"
      "s_waitcnt vmcnt(8)\n\t"
      "LW92%=:\n\t"
      "s_waitcnt vmcnt(9)\n\t"
      "s_barrier\n\t"
      "s_cmp_lg_u32 s48, s60\n\t"
      "s_cbranch_scc1 LP2%=\n\t"
      "s_barrier\n\t"
      "LP2%=:\n\t"
      "s_add_u32 s48, s48, 1\n\t"
      "s_cmp_eq_u32 s5, 0\n\t"
      "s_cbranch_scc1 LKA%=\n\t"
      "s_add_u32 m0, s49, s55\n\t"
      "s_add_u32 s15, s59, 4096\n\t"
      "buffer_load_dwordx4 v10, s[36:39], s59 offen nt lds\n\t"
      "buffer_load_dwordx4 v11, s[36:39], s59 offen offset:1024 nt lds\n\t"
      "buffer_load_dwordx4 v12, s[36:39], s59 offen offset:2048 nt lds\n\t"
      "buffer_load_dwordx4 v13, s[36:39], s59 offen offset:3072 nt lds\n\t"
      "s_add_u32 m0, m0, 4096\n\t"
      "s_nop 0\n\t"
      "buffer_load_dwordx4 v10, s[36:39], s15 offen nt lds\n\t"
      "buffer_load_dwordx4 v11, s[36:39], s15 offen offset:1024 nt lds\n\t"
      "buffer_load_dwordx4 v12, s[36:39], s15 offen offset:2048 nt lds\n\t"
      "buffer_load_dwordx4 v13, s[36:39], s15 offen offset:3072 nt lds\n\t"
      "s_add_u32 s49, s49, 36864\n\t"
      "s_cmp_eq_u32 s49, 147456\n\t"
      "s_cselect_b32 s49, 0, s49\n\t"
      "s_branch LLNK%=\n\t"
      "LKA%=:\n\t"
      "s_add_u32 m0, s49, s55\n\t"
      "s_add_u32 s15, s59, 4096\n\t"
      "buffer_load_dwordx4 v10, s[36:39], s59 offen lds\n\t"
      "buffer_load_dwordx4 v11, s[36:39], s59 offen offset:1024 lds\n\t"
      "buffer_load_dwordx4 v12, s[36:39], s59 offen offset:2048 lds\n\t"
      "buffer_load_dwordx4 v13, s[36:39], s59 offen offset:3072 lds\n\t"
      "s_add_u32 m0, m0, 4096\n\t"
      "s_nop 0\n\t"
      "buffer_load_dwordx4 v10, s[36:39], s15 offen lds\n\t"
      "buffer_load_dwordx4 v11, s[36:39], s15 offen offset:1024 lds\n\t"
      "buffer_load_dwordx4 v12, s[36:39], s15 offen offset:2048 lds\n\t"
      "buffer_load_dwordx4 v13, s[36:39], s15 offen offset:3072 lds\n\t"
      "s_add_u32 s49, s49, 36864\n\t"
      "s_cmp_eq_u32 s49, 147456\n\t"
      "s_cselect_b32 s49, 0, s49\n\t"
      "LLNK%=:\n\t"
      "s_bfe_u32 s14, s13, 0x10001\n\t"
      "s_cmp_lg_u32 s14, s52\n\t"
      "s_cbranch_scc1 LLNV0%=\n\t"
      "s_cmp_eq_u32 s48, s35\n\t"
      "s_cbranch_scc1 LW93%=\n\t"
      "s_waitcnt vmcnt(8)\n\t"
      "LW93%=:\n\t"
      "s_waitcnt vmcnt(9)\n\t"
      "s_barrier\n\t"
      "s_cmp_lg_u32 s48, s60\n\t"
      "s_cbranch_scc1 LP3%=\n\t"
      "s_barrier\n\t"
      "LP3%=:\n\t"
      "s_add_u32 s48, s48, 1\n\t"
      "s_add_u32 m0, s49, s55\n\t"
      "s_add_u32 s15, s59, 4096\n\t"
      "buffer_load_dwordx4 v10, s[40:43], s59 offen lds\n\t"
      "buffer_load_dwordx4 v11, s[40:43], s59 offen offset:1024 lds\n\t"
      "buffer_load_dwordx4 v12, s[40:43], s59 offen offset:2048 lds\n\t"
      "buffer_load_dwordx4 v13, s[40:43], s59 offen offset:3072 lds\n\t"
      "s_add_u32 m0, m0, 4096\n\t"
      "s_nop 0\n\t"
      "buffer_load_dwordx4 v10, s[40:43], s15 offen lds\n\t"
      "buffer_load_dwordx4 v11, s[40:43], s15 offen offset:1024 lds\n\t"
      "buffer_load_dwordx4 v12, s[40:43], s15 offen offset:2048 lds\n\t"
      "buffer_load_dwordx4 v13, s[40:43], s15 offen offset:3072 lds\n\t"
      "s_add_u32 s49, s49, 36864\n\t"
      "s_cmp_eq_u32 s49, 147456\n\t"
      "s_cselect_b32 s49, 0, s49\n\t"
      "LLNV0%=:\n\t"
      "s_lshr_b32 s14, s13, 2\n\t"
      "s_cmp_lg_u32 s14, s52\n\t"
      "s_cbranch_scc1 LLNV%=\n\t"
      "s_cmp_eq_u32 s48, s35\n\t"
      "s_cbranch_scc1 LW94%=\n\t"
      "s_waitcnt vmcnt(8)\n\t"
      "LW94%=:\n\t"
      "s_waitcnt vmcnt(9)\n\t"
      "s_barrier\n\t"
      "s_cmp_lg_u32 s48, s60\n\t"
      "s_cbranch_scc1 LP4%=\n\t"
      "s_barrier\n\t"
      "LP4%=:\n\t"
      "s_add_u32 s48, s48, 1\n\t"
      "s_add_u32 m0, s49, s56\n\t"
      "s_mul_i32 s14, s53, 147456\n\t"
      "buffer_load_dwordx4 v14, s[44:47], s14 offen lds\n\t"
      "buffer_load_dwordx4 v15, s[44:47], s14 offen offset:1024 lds\n\t"
      "buffer_load_dwordx4 v16, s[44:47], s14 offen offset:2048 lds\n\t"
      "s_add_u32 m0, m0, 3072\n\t"
      "s_nop 0\n\t"
      "buffer_load_dwordx4 v17, s[44:47], s14 offen lds\n\t"
      "buffer_load_dwordx4 v18, s[44:47], s14 offen offset:1024 lds\n\t"
      "buffer_load_dwordx4 v19, s[44:47], s14 offen offset:2048 lds\n\t"
      "s_add_u32 m0, m0, 3072\n\t"
      "s_nop 0\n\t"
      "buffer_load_dwordx4 v20, s[44:47], s14 offen lds\n\t"
      "buffer_load_dwordx4 v21, s[44:47], s14 offen offset:1024 lds\n\t"
      "buffer_load_dwordx4 v22, s[44:47], s14 offen offset:2048 lds\n\t"
      "s_add_u32 s49, s49, 36864\n\t"
      "s_cmp_eq_u32 s49, 147456\n\t"
      "s_cselect_b32 s49, 0, s49\n\t"
      "LLNV%=:\n\t"
      "s_cmp_lg_u32 s52, 0\n\t"
      "s_cbranch_scc1 LLT%=\n\t"
      "s_mov_b32 s52, 1\n\t"
      "s_sub_u32 s51, 9, s12\n\t"
      "s_cmp_eq_u32 s51, 0\n\t"
      "s_cbranch_scc1 LLI%=\n\t"
      "s_branch LLQ%=\n\t"
      "LLT%=:\n\t"
      "s_and_b32 s53, s17, 3\n\t"
      "s_lshr_b32 s17, s17, 2\n\t"
      "s_add_u32 s53, s53, s10\n\t"
      "s_and_b32 s53, s53, 3\n\t"
      "s_add_u32 s14, s57, s53\n\t"
      "s_lshr_b32 s14, s14, s58\n\t"
      "s_lshl_b32 s14, s14, 13\n\t"
      "s_bitcmp1_b32 s13, 2\n\t"
      "s_cbranch_scc1 LX9%=\n\t"
      "s_waitcnt vmcnt(8)\n\t"
      "LX9%=:\n\t"
      "s_waitcnt vmcnt(9)\n\t"
      "s_barrier\n\t"
      "s_cmp_lg_u32 s48, s60\n\t"
      "s_cbranch_scc1 LP5%=\n\t"
      "s_barrier\n\t"
      "LP5%=:\n\t"
      "s_add_u32 s48, s48, 1\n\t"
      "s_add_u32 m0, s49, s55\n\t"
      "s_add_u32 s15, s14, 4096\n\t"
      "buffer_load_dwordx4 v10, s[40:43], s14 offen lds\n\t"
      "buffer_load_dwordx4 v11, s[40:43], s14 offen offset:1024 lds\n\t"
      "buffer_load_dwordx4 v12, s[40:43], s14 offen offset:2048 lds\n\t"
      "buffer_load_dwordx4 v13, s[40:43], s14 offen offset:3072 lds\n\t"
      "s_add_u32 m0, m0, 4096\n\t"
      "s_nop 0\n\t"
      "buffer_load_dwordx4 v10, s[40:43], s15 offen lds\n\t"
      "buffer_load_dwordx4 v11, s[40:43], s15 offen offset:1024 lds\n\t"
      "buffer_load_dwordx4 v12, s[40:43], s15 offen offset:2048 lds\n\t"
      "buffer_load_dwordx4 v13, s[40:43], s15 offen offset:3072 lds\n\t"
      "s_add_u32 s49, s49, 36864\n\t"
      "s_cmp_eq_u32 s49, 147456\n\t"
      "s_cselect_b32 s49, 0, s49\n\t"
      "s_cmp_eq_u32 s48, s35\n\t"
      "s_cbranch_scc1 LW96%=\n\t"
      "s_waitcnt vmcnt(8)\n\t"
      "LW96%=:\n\t"
      "s_waitcnt vmcnt(9)\n\t"
      "s_barrier\n\t"
      "s_cmp_lg_u32 s48, s60\n\t"
      "s_cbranch_scc1 LP6%=\n\t"
      "s_barrier\n\t"
      "LP6%=:\n\t"
      "s_add_u32 s48, s48, 1\n\t"
      "s_add_u32 m0, s49, s56\n\t"
      "s_mul_i32 s14, s53, 147456\n\t"
      "buffer_load_dwordx4 v14, s[44:47], s14 offen lds\n\t"
      "buffer_load_dwordx4 v15, s[44:47], s14 offen offset:1024 lds\n\t"
      "buffer_load_dwordx4 v16, s[44:47], s14 offen offset:2048 lds\n\t"
      "s_add_u32 m0, m0, 3072\n\t"
      "s_nop 0\n\t"
      "buffer_load_dwordx4 v17, s[44:47], s14 offen lds\n\t"
      "buffer_load_dwordx4 v18, s[44:47], s14 offen offset:1024 lds\n\t"
      "buffer_load_dwordx4 v19, s[44:47], s14 offen offset:2048 lds\n\t"
      "s_add_u32 m0, m0, 3072\n\t"
      "s_nop 0\n\t"
      "buffer_load_dwordx4 v20, s[44:47], s14 offen lds\n\t"
      "buffer_load_dwordx4 v21, s[44:47], s14 offen offset:1024 lds\n\t"
      "buffer_load_dwordx4 v22, s[44:47], s14 offen offset:2048 lds\n\t"
      "s_add_u32 s49, s49, 36864\n\t"
      "s_cmp_eq_u32 s49, 147456\n\t"
      "s_cselect_b32 s49, 0, s49\n\t"
      "s_and_b32 s53, s17, 3\n\t"
      "s_lshr_b32 s17, s17, 2\n\t"
      "s_add_u32 s53, s53, s10\n\t"
      "s_and_b32 s53, s53, 3\n\t"
      "s_add_u32 s14, s57, s53\n\t"
      "s_lshr_b32 s14, s14, s58\n\t"
      "s_lshl_b32 s14, s14, 13\n\t"
      "s_add_u32 m0, s49, s55\n\t"
      "s_add_u32 s15, s14, 4096\n\t"
      "buffer_load_dwordx4 v10, s[40:43], s14 offen lds\n\t"
      "buffer_load_dwordx4 v11, s[40:43], s14 offen offset:1024 lds\n\t"
      "buffer_load_dwordx4 v12, s[40:43], s14 offen offset:2048 lds\n\t"
      "buffer_load_dwordx4 v13, s[40:43], s14 offen offset:3072 lds\n\t"
      "s_add_u32 m0, m0, 4096\n\t"
      "s_nop 0\n\t"
      "buffer_load_dwordx4 v10, s[40:43], s15 offen lds\n\t"
      "buffer_load_dwordx4 v11, s[40:43], s15 offen offset:1024 lds\n\t"
      "buffer_load_dwordx4 v12, s[40:43], s15 offen offset:2048 lds\n\t"
      "buffer_load_dwordx4 v13, s[40:43], s15 offen offset:3072 lds\n\t"
      "s_add_u32 s49, s49, 36864\n\t"
      "s_cmp_eq_u32 s49, 147456\n\t"
      "s_cselect_b32 s49, 0, s49\n\t"
      "s_waitcnt vmcnt(8)\n\t"
      "s_barrier\n\t"
      "s_add_u32 m0, s49, s56\n\t"
      "s_mul_i32 s14, s53, 147456\n\t"
      "buffer_load_dwordx4 v14, s[44:47], s14 offen lds\n\t"
      "buffer_load_dwordx4 v15, s[44:47], s14 offen offset:1024 lds\n\t"
      "buffer_load_dwordx4 v16, s[44:47], s14 offen offset:2048 lds\n\t"
      "s_add_u32 m0, m0, 3072\n\t"
      "s_nop 0\n\t"
      "buffer_load_dwordx4 v17, s[44:47], s14 offen lds\n\t"
      "buffer_load_dwordx4 v18, s[44:47], s14 offen offset:1024 lds\n\t"
      "buffer_load_dwordx4 v19, s[44:47], s14 offen offset:2048 lds\n\t"
      "s_add_u32 m0, m0, 3072\n\t"
      "s_nop 0\n\t"
      "buffer_load_dwordx4 v20, s[44:47], s14 offen lds\n\t"
      "buffer_load_dwordx4 v21, s[44:47], s14 offen offset:1024 lds\n\t"
      "buffer_load_dwordx4 v22, s[44:47], s14 offen offset:2048 lds\n\t"
      "s_add_u32 s49, s49, 36864\n\t"
      "s_cmp_eq_u32 s49, 147456\n\t"
      "s_cselect_b32 s49, 0, s49\n\t"
      "s_waitcnt vmcnt(0)\n\t"
      "s_barrier\n\t"
      "s_and_b32 s53, s17, 3\n\t"
      "s_lshr_b32 s17, s17, 2\n\t"
      "s_add_u32 s53, s53, s10\n\t"
      "s_and_b32 s53, s53, 3\n\t"
      "s_add_u32 s14, s57, s53\n\t"
      "s_lshr_b32 s14, s14, s58\n\t"
      "s_lshl_b32 s14, s14, 13\n\t"
      "s_add_u32 m0, s49, s55\n\t"
      "s_add_u32 s15, s14, 4096\n\t"
      "buffer_load_dwordx4 v10, s[40:43], s14 offen lds\n\t"
      "buffer_load_dwordx4 v11, s[40:43], s14 offen offset:1024 lds\n\t"
      "buffer_load_dwordx4 v12, s[40:43], s14 offen offset:2048 lds\n\t"
      "buffer_load_dwordx4 v13, s[40:43], s14 offen offset:3072 lds\n\t"
      "s_add_u32 m0, m0, 4096\n\t"
      "s_nop 0\n\t"
      "buffer_load_dwordx4 v10, s[40:43], s15 offen lds\n\t"
      "buffer_load_dwordx4 v11, s[40:43], s15 offen offset:1024 lds\n\t"
      "buffer_load_dwordx4 v12, s[40:43], s15 offen offset:2048 lds\n\t"
      "buffer_load_dwordx4 v13, s[40:43], s15 offen offset:3072 lds\n\t"
      "s_add_u32 s49, s49, 36864\n\t"
      "s_cmp_eq_u32 s49, 147456\n\t"
      "s_cselect_b32 s49, 0, s49\n\t"
      "s_add_u32 m0, s49, s56\n\t"
      "s_mul_i32 s14, s53, 147456\n\t"
      "buffer_load_dwordx4 v14, s[44:47], s14 offen lds\n\t"
      "buffer_load_dwordx4 v15, s[44:47], s14 offen offset:1024 lds\n\t"
      "buffer_load_dwordx4 v16, s[44:47], s14 offen offset:2048 lds\n\t"
      "s_add_u32 m0, m0, 3072\n\t"
      "s_nop 0\n\t"
      "buffer_load_dwordx4 v17, s[44:47], s14 offen lds\n\t"
      "buffer_load_dwordx4 v18, s[44:47], s14 offen offset:1024 lds\n\t"
      "buffer_load_dwordx4 v19, s[44:47], s14 offen offset:2048 lds\n\t"
      "s_add_u32 m0, m0, 3072\n\t"
      "s_nop 0\n\t"
      "buffer_load_dwordx4 v20, s[44:47], s14 offen lds\n\t"
      "buffer_load_dwordx4 v21, s[44:47], s14 offen offset:1024 lds\n\t"
      "buffer_load_dwordx4 v22, s[44:47], s14 offen offset:2048 lds\n\t"
      "s_add_u32 s49, s49, 36864\n\t"
      "s_cmp_eq_u32 s49, 147456\n\t"
      "s_cselect_b32 s49, 0, s49\n\t"
      "s_waitcnt vmcnt(0)\n\t"
      "s_barrier\n\t"
      "LLD%=:\n\t"
      "s_lshl2_add_u32 s14, s5, s6\n\t"
      "s_lshl2_add_u32 s14, s14, s11\n\t"
      "s_mul_i32 s14, s14, 335872\n\t"
      "s_add_u32 s14, s14, 8388608\n\t"
      "s_add_u32 s36, s30, s14\n\t"
      "s_addc_u32 s37, s31, 0\n\t"
      "s_mov_b32 s38, 335872\n\t"
      "s_mov_b32 s39, 0x20000\n\t"
      "v_mov_b32 v2, 147456\n\t"
      "ds_read_b64 v[4:5], v2\n\t"
      "v_lshlrev_b32 v13, 4, v1\n\t"
      "v_lshlrev_b32 v14, 3, v1\n\t"
      "s_waitcnt lgkmcnt(0)\n\t"
      "v_readfirstlane_b32 s40, v4\n\t"
      "v_readfirstlane_b32 s41, v5\n\t"
      "s_branch LRED%=\n\t"
      "LCOMP%=:\n\t"
      "s_setprio 2\n\t"
      "s_lshr_b32 s9, s8, 2\n\t"
      "s_and_b32 s10, s8, 3\n\t"
      "s_add_u32 s11, s10, s6\n\t"
      "s_and_b32 s11, s11, 3\n\t"
      "v_and_b32 v242, 15, v1\n\t"
      "v_lshrrev_b32 v243, 4, v1\n\t"
      "v_bfe_u32 v244, v1, 2, 2\n\t"
      "v_and_b32 v245, 3, v1\n\t"
      "v_and_b32 v246, 1, v243\n\t"
      "v_lshrrev_b32 v249, 1, v243\n\t"
      "v_lshrrev_b32 v247, 1, v244\n\t"
      "v_lshl_or_b32 v247, v246, 1, v247\n\t"
      "v_lshlrev_b32 v247, 5, v247\n\t"
      "v_lshl_or_b32 v247, v245, 3, v247\n\t"
      "v_lshl_or_b32 v247, v244, 7, v247\n\t"
      "v_lshl_or_b32 v247, v246, 10, v247\n\t"
      "v_lshl_or_b32 v2, v249, 12, v247\n\t"
      "v_xor_b32 v3, 32, v2\n\t"
      "v_xor_b32 v4, 64, v2\n\t"
      "v_xor_b32 v5, 96, v2\n\t"
      "v_lshrrev_b32 v247, 1, v242\n\t"
      "v_xor_b32 v247, v243, v247\n\t"
      "v_lshlrev_b32 v247, 4, v247\n\t"
      "v_lshl_or_b32 v6, v242, 7, v247\n\t"
      "s_lshl_b32 s14, s4, 12\n\t"
      "s_add_u32 s14, s14, 16384\n\t"
      "v_add_u32 v6, s14, v6\n\t"
      "v_xor_b32 v7, 64, v6\n\t"
      "v_xor_b32 v247, v249, v242\n\t"
      "v_lshlrev_b32 v247, 4, v247\n\t"
      "v_lshl_or_b32 v247, v246, 3, v247\n\t"
      "v_lshl_or_b32 v11, v242, 8, v247\n\t"
      "s_lshl_b32 s14, s4, 13\n\t"
      "v_add_u32 v11, s14, v11\n\t"
      "v_lshl_add_u32 v247, v243, 2, v244\n\t"
      "v_mul_u32_u24 v247, 288, v247\n\t"
      "v_lshl_add_u32 v12, v245, 3, v247\n\t"
      "v_lshlrev_b32 v13, 4, v1\n\t"
      "v_lshlrev_b32 v14, 3, v1\n\t"
      "v_mov_b32 v16, 0\n\t"
      "v_mov_b32 v17, 0\n\t"
      "v_mov_b32 v18, 0\n\t"
      "v_mov_b32 v19, 0\n\t"
      "v_mov_b32 v20, 0\n\t"
      "v_mov_b32 v21, 0\n\t"
      "v_mov_b32 v22, 0\n\t"
      "v_mov_b32 v23, 0\n\t"
      "v_mov_b32 v24, 0\n\t"
      "v_mov_b32 v25, 0\n\t"
      "v_mov_b32 v26, 0\n\t"
      "v_mov_b32 v27, 0\n\t"
      "v_mov_b32 v28, 0\n\t"
      "v_mov_b32 v29, 0\n\t"
      "v_mov_b32 v30, 0\n\t"
      "v_mov_b32 v31, 0\n\t"
      "v_mov_b32 v32, 0\n\t"
      "v_mov_b32 v33, 0\n\t"
      "v_mov_b32 v34, 0\n\t"
      "v_mov_b32 v35, 0\n\t"
      "v_mov_b32 v36, 0\n\t"
      "v_mov_b32 v37, 0\n\t"
      "v_mov_b32 v38, 0\n\t"
      "v_mov_b32 v39, 0\n\t"
      "v_mov_b32 v40, 0\n\t"
      "v_mov_b32 v41, 0\n\t"
      "v_mov_b32 v42, 0\n\t"
      "v_mov_b32 v43, 0\n\t"
      "v_mov_b32 v44, 0\n\t"
      "v_mov_b32 v45, 0\n\t"
      "v_mov_b32 v46, 0\n\t"
      "v_mov_b32 v47, 0\n\t"
      "v_mov_b32 v48, 0\n\t"
      "v_mov_b32 v49, 0\n\t"
      "v_mov_b32 v50, 0\n\t"
      "v_mov_b32 v51, 0\n\t"
      "v_mov_b32 v52, 0\n\t"
      "v_mov_b32 v53, 0\n\t"
      "v_mov_b32 v54, 0\n\t"
      "v_mov_b32 v55, 0\n\t"
      "v_mov_b32 v56, 0\n\t"
      "v_mov_b32 v57, 0\n\t"
      "v_mov_b32 v58, 0\n\t"
      "v_mov_b32 v59, 0\n\t"
      "v_mov_b32 v60, 0\n\t"
      "v_mov_b32 v61, 0\n\t"
      "v_mov_b32 v62, 0\n\t"
      "v_mov_b32 v63, 0\n\t"
      "v_mov_b32 v64, 0\n\t"
      "v_mov_b32 v65, 0\n\t"
      "v_mov_b32 v66, 0\n\t"
      "v_mov_b32 v67, 0\n\t"
      "v_mov_b32 v68, 0\n\t"
      "v_mov_b32 v69, 0\n\t"
      "v_mov_b32 v70, 0\n\t"
      "v_mov_b32 v71, 0\n\t"
      "v_mov_b32 v72, 0\n\t"
      "v_mov_b32 v73, 0\n\t"
      "v_mov_b32 v74, 0\n\t"
      "v_mov_b32 v75, 0\n\t"
      "v_mov_b32 v76, 0\n\t"
      "v_mov_b32 v77, 0\n\t"
      "v_mov_b32 v78, 0\n\t"
      "v_mov_b32 v79, 0\n\t"
      "s_mov_b32 s48, 0\n\t"
      "s_mov_b32 s49, 0\n\t"
      "s_mov_b32 s52, 0\n\t"
      "s_mov_b32 s51, s12\n\t"
      "s_mov_b32 s55, 0x3e0293ee\n\t"
      "s_mov_b32 s59, 36864\n\t"
      "s_mov_b32 s60, -110592\n\t"
      "s_lshl_b32 s58, s10, 14\n\t"
      "s_bfe_u32 s14, s17, 131074\n\t"
      "s_add_u32 s14, s14, s10\n\t"
      "s_and_b32 s14, s14, 3\n\t"
      "s_lshl_b32 s61, s14, 14\n\t"
      "s_bfe_u32 s14, s17, 131076\n\t"
      "s_add_u32 s14, s14, s10\n\t"
      "s_and_b32 s14, s14, 3\n\t"
      "s_lshl_b32 s62, s14, 14\n\t"
      "s_bfe_u32 s14, s17, 131078\n\t"
      "s_add_u32 s14, s14, s10\n\t"
      "s_and_b32 s14, s14, 3\n\t"
      "s_lshl_b32 s63, s14, 14\n\t"
      "s_lshl4_add_u32 s56, s4, s9\n\t"
      "s_mul_i32 s56, s56, 5248\n\t"
      "s_add_u32 s57, s56, 41984\n\t"
      "s_waitcnt lgkmcnt(0)\n\t"
      "s_lshl2_add_u32 s14, s5, s6\n\t"
      "s_lshl_b32 s16, s14, 7\n\t"
      "v_mov_b32 v204, s16\n\t"
      "s_lshl3_add_u32 s14, s14, s9\n\t"
      "s_lshl_b32 s14, s14, 16\n\t"
      "s_lshl_b32 s15, s4, 12\n\t"
      "s_add_u32 s14, s14, s15\n\t"
      "s_add_u32 s14, s14, 1048576\n\t"
      "s_add_u32 s44, s30, s14\n\t"
      "s_addc_u32 s45, s31, 0\n\t"
      "s_mov_b32 s46, 65536\n\t"
      "s_mov_b32 s47, 0x20000\n\t"
      "s_lshl2_add_u32 s14, s5, s6\n\t"
      "s_lshl2_add_u32 s14, s14, s11\n\t"
      "s_mul_i32 s14, s14, 335872\n\t"
      "s_add_u32 s14, s14, 8388608\n\t"
      "s_add_u32 s36, s30, s14\n\t"
      "s_addc_u32 s37, s31, 0\n\t"
      "s_mov_b32 s38, 335872\n\t"
      "s_mov_b32 s39, 0x20000\n\t"
      "s_add_u32 s64, s36, s56\n\t"
      "s_addc_u32 s65, s37, 0\n\t"
      "s_lshl_b32 s14, s4, 1\n\t"
      "s_add_u32 s14, s14, 0\n\t"
      "s_cmp_eq_u32 s14, s9\n\t"
      "s_cselect_b32 s66, 0, 5248\n\t"
      "s_mov_b32 s67, 0x20000\n\t"
      "s_add_u32 s68, s36, s57\n\t"
      "s_addc_u32 s69, s37, 0\n\t"
      "s_lshl_b32 s14, s4, 1\n\t"
      "s_add_u32 s14, s14, 1\n\t"
      "s_cmp_eq_u32 s14, s9\n\t"
      "s_cselect_b32 s70, 0, 5248\n\t"
      "s_mov_b32 s71, 0x20000\n\t"
      "s_movk_i32 s56, 4096\n\t"
      "s_barrier\n\t"
      "global_load_dwordx2 v[206:207], v204, s[30:31] sc1\n\t"
      "LSEG%=:\n\t"
      "s_cmp_eq_u32 s51, 0\n\t"
      "s_cbranch_scc1 LQDONE%=\n\t"
      "s_sub_u32 s51, s51, 1\n\t"
      "s_sub_u32 s50, s48, s49\n\t"
      "s_mul_i32 s50, s50, 36864\n\t"
      "v_add_u32 v2, s50, v2\n\t"
      "v_add_u32 v3, s50, v3\n\t"
      "v_add_u32 v4, s50, v4\n\t"
      "v_add_u32 v5, s50, v5\n\t"
      "v_add_u32 v6, s50, v6\n\t"
      "v_add_u32 v7, s50, v7\n\t"
      "s_waitcnt lgkmcnt(0)\n\t"
      "s_barrier\n\t"
      "ds_read_b64_tr_b16 v[88:89], v2 offset:0\n\t"
      "ds_read_b64_tr_b16 v[90:91], v2 offset:512\n\t"
      "ds_read_b128 v[120:123], v6\n\t"
      "ds_read_b64_tr_b16 v[92:93], v3 offset:0\n\t"
      "ds_read_b64_tr_b16 v[94:95], v3 offset:512\n\t"
      "ds_read_b128 v[124:127], v6 offset:2048\n\t"
      "ds_read_b64_tr_b16 v[96:97], v4 offset:0\n\t"
      "ds_read_b64_tr_b16 v[98:99], v4 offset:512\n\t"
      "ds_read_b64_tr_b16 v[100:101], v5 offset:0\n\t"
      "ds_read_b64_tr_b16 v[102:103], v5 offset:512\n\t"
      "ds_read_b64_tr_b16 v[104:105], v2 offset:2048\n\t"
      "ds_read_b64_tr_b16 v[106:107], v2 offset:2560\n\t"
      "ds_read_b64_tr_b16 v[108:109], v3 offset:2048\n\t"
      "ds_read_b64_tr_b16 v[110:111], v3 offset:2560\n\t"
      "s_branch LQ%=\n\t"
      "LQB%=:\n\t"
      "s_barrier\n\t"
      "s_waitcnt lgkmcnt(8)\n\t"
      "v_mfma_f32_16x16x32_bf16 v[32:35], v[96:99], v[128:131], v[32:35]\n\t"
      "ds_read_b64_tr_b16 v[88:89], v2 offset:0\n\t"
      "v_mfma_f32_16x16x32_bf16 v[36:39], v[96:99], v[132:135], v[36:39]\n\t"
      "ds_read_b64_tr_b16 v[90:91], v2 offset:512\n\t"
      "ds_read_b128 v[120:123], v6\n\t"
      "v_add_u32 v5, s50, v5\n\t"
      "v_mfma_f32_16x16x32_bf16 v[40:43], v[100:103], v[128:131], v[40:43]\n\t"
      "ds_read_b64_tr_b16 v[92:93], v3 offset:0\n\t"
      "v_mfma_f32_16x16x32_bf16 v[44:47], v[100:103], v[132:135], v[44:47]\n\t"
      "ds_read_b64_tr_b16 v[94:95], v3 offset:512\n\t"
      "ds_read_b128 v[124:127], v6 offset:2048\n\t"
      "s_waitcnt lgkmcnt(10)\n\t"
      "v_mfma_f32_16x16x32_bf16 v[48:51], v[104:107], v[128:131], v[48:51]\n\t"
      "ds_read_b64_tr_b16 v[96:97], v4 offset:0\n\t"
      "v_mfma_f32_16x16x32_bf16 v[52:55], v[104:107], v[132:135], v[52:55]\n\t"
      "ds_read_b64_tr_b16 v[98:99], v4 offset:512\n\t"
      "v_mfma_f32_16x16x32_bf16 v[56:59], v[108:111], v[128:131], v[56:59]\n\t"
      "ds_read_b64_tr_b16 v[100:101], v5 offset:0\n\t"
      "v_mfma_f32_16x16x32_bf16 v[60:63], v[108:111], v[132:135], v[60:63]\n\t"
      "ds_read_b64_tr_b16 v[102:103], v5 offset:512\n\t"
      "s_waitcnt lgkmcnt(10)\n\t"
      "v_mfma_f32_16x16x32_bf16 v[64:67], v[112:115], v[128:131], v[64:67]\n\t"
      "ds_read_b64_tr_b16 v[104:105], v2 offset:2048\n\t"
      "v_mfma_f32_16x16x32_bf16 v[68:71], v[112:115], v[132:135], v[68:71]\n\t"
      "ds_read_b64_tr_b16 v[106:107], v2 offset:2560\n\t"
      "v_mfma_f32_16x16x32_bf16 v[72:75], v[116:119], v[128:131], v[72:75]\n\t"
      "ds_read_b64_tr_b16 v[108:109], v3 offset:2048\n\t"
      "v_mfma_f32_16x16x32_bf16 v[76:79], v[116:119], v[132:135], v[76:79]\n\t"
      "ds_read_b64_tr_b16 v[110:111], v3 offset:2560\n\t"
      "LQ%=:\n\t"
      "s_waitcnt lgkmcnt(8)\n\t"
      "v_mfma_f32_16x16x32_bf16 v[16:19], v[88:91], v[120:123], v[16:19]\n\t"
      "ds_read_b64_tr_b16 v[112:113], v4 offset:2048\n\t"
      "v_mfma_f32_16x16x32_bf16 v[20:23], v[88:91], v[124:127], v[20:23]\n\t"
      "ds_read_b64_tr_b16 v[114:115], v4 offset:2560\n\t"
      "v_mfma_f32_16x16x32_bf16 v[24:27], v[92:95], v[120:123], v[24:27]\n\t"
      "ds_read_b64_tr_b16 v[116:117], v5 offset:2048\n\t"
      "s_add_u32 s48, s48, 1\n\t"
      "v_mfma_f32_16x16x32_bf16 v[28:31], v[92:95], v[124:127], v[28:31]\n\t"
      "ds_read_b64_tr_b16 v[118:119], v5 offset:2560\n\t"
      "s_and_b32 s48, s48, 3\n\t"
      "s_waitcnt lgkmcnt(8)\n\t"
      "v_mfma_f32_16x16x32_bf16 v[32:35], v[96:99], v[120:123], v[32:35]\n\t"
      "ds_read_b64_tr_b16 v[88:89], v2 offset:8192\n\t"
      "v_mfma_f32_16x16x32_bf16 v[36:39], v[96:99], v[124:127], v[36:39]\n\t"
      "ds_read_b64_tr_b16 v[90:91], v2 offset:8704\n\t"
      "ds_read_b128 v[128:131], v7\n\t"
      "v_mfma_f32_16x16x32_bf16 v[40:43], v[100:103], v[120:123], v[40:43]\n\t"
      "ds_read_b64_tr_b16 v[92:93], v3 offset:8192\n\t"
      "s_cselect_b32 s50, s59, s60\n\t"
      "v_mfma_f32_16x16x32_bf16 v[44:47], v[100:103], v[124:127], v[44:47]\n\t"
      "ds_read_b64_tr_b16 v[94:95], v3 offset:8704\n\t"
      "ds_read_b128 v[132:135], v7 offset:2048\n\t"
      "s_waitcnt lgkmcnt(10)\n\t"
      "v_mfma_f32_16x16x32_bf16 v[48:51], v[104:107], v[120:123], v[48:51]\n\t"
      "ds_read_b64_tr_b16 v[96:97], v4 offset:8192\n\t"
      "v_mfma_f32_16x16x32_bf16 v[52:55], v[104:107], v[124:127], v[52:55]\n\t"
      "ds_read_b64_tr_b16 v[98:99], v4 offset:8704\n\t"
      "v_add_u32 v6, s50, v6\n\t"
      "v_mfma_f32_16x16x32_bf16 v[56:59], v[108:111], v[120:123], v[56:59]\n\t"
      "ds_read_b64_tr_b16 v[100:101], v5 offset:8192\n\t"
      "v_mfma_f32_16x16x32_bf16 v[60:63], v[108:111], v[124:127], v[60:63]\n\t"
      "ds_read_b64_tr_b16 v[102:103], v5 offset:8704\n\t"
      "v_add_u32 v7, s50, v7\n\t"
      "s_waitcnt lgkmcnt(10)\n\t"
      "v_mfma_f32_16x16x32_bf16 v[64:67], v[112:115], v[120:123], v[64:67]\n\t"
      "ds_read_b64_tr_b16 v[104:105], v2 offset:10240\n\t"
      "v_mfma_f32_16x16x32_bf16 v[68:71], v[112:115], v[124:127], v[68:71]\n\t"
      "ds_read_b64_tr_b16 v[106:107], v2 offset:10752\n\t"
      "v_mfma_f32_16x16x32_bf16 v[72:75], v[116:119], v[120:123], v[72:75]\n\t"
      "ds_read_b64_tr_b16 v[108:109], v3 offset:10240\n\t"
      "v_mfma_f32_16x16x32_bf16 v[76:79], v[116:119], v[124:127], v[76:79]\n\t"
      "ds_read_b64_tr_b16 v[110:111], v3 offset:10752\n\t"
      "v_add_u32 v2, s50, v2\n\t"
      "s_waitcnt lgkmcnt(8)\n\t"
      "v_mfma_f32_16x16x32_bf16 v[16:19], v[88:91], v[128:131], v[16:19]\n\t"
      "ds_read_b64_tr_b16 v[112:113], v4 offset:10240\n\t"
      "v_mfma_f32_16x16x32_bf16 v[20:23], v[88:91], v[132:135], v[20:23]\n\t"
      "ds_read_b64_tr_b16 v[114:115], v4 offset:10752\n\t"
      "v_add_u32 v3, s50, v3\n\t"
      "v_mfma_f32_16x16x32_bf16 v[24:27], v[92:95], v[128:131], v[24:27]\n\t"
      "ds_read_b64_tr_b16 v[116:117], v5 offset:10240\n\t"
      "v_add_u32 v4, s50, v4\n\t"
      "v_mfma_f32_16x16x32_bf16 v[28:31], v[92:95], v[132:135], v[28:31]\n\t"
      "ds_read_b64_tr_b16 v[118:119], v5 offset:10752\n\t"
      "s_sub_u32 s51, s51, 1\n\t"
      "s_cbranch_scc0 LQB%=\n\t"
      "s_waitcnt lgkmcnt(8)\n\t"
      "v_mfma_f32_16x16x32_bf16 v[32:35], v[96:99], v[128:131], v[32:35]\n\t"
      "v_add_u32 v5, s50, v5\n\t"
      "v_mfma_f32_16x16x32_bf16 v[36:39], v[96:99], v[132:135], v[36:39]\n\t"
      "v_mfma_f32_16x16x32_bf16 v[40:43], v[100:103], v[128:131], v[40:43]\n\t"
      "v_mfma_f32_16x16x32_bf16 v[44:47], v[100:103], v[132:135], v[44:47]\n\t"
      "s_waitcnt lgkmcnt(4)\n\t"
      "v_mfma_f32_16x16x32_bf16 v[48:51], v[104:107], v[128:131], v[48:51]\n\t"
      "v_cvt_pk_bf16_f32 v88, v16, v17\n\t"
      "v_cvt_pk_bf16_f32 v89, v18, v19\n\t"
      "v_mfma_f32_16x16x32_bf16 v[52:55], v[104:107], v[132:135], v[52:55]\n\t"
      "v_cvt_pk_bf16_f32 v90, v24, v25\n\t"
      "v_cvt_pk_bf16_f32 v91, v26, v27\n\t"
      "v_mfma_f32_16x16x32_bf16 v[56:59], v[108:111], v[128:131], v[56:59]\n\t"
      "v_cvt_pk_bf16_f32 v92, v20, v21\n\t"
      "v_cvt_pk_bf16_f32 v93, v22, v23\n\t"
      "v_mfma_f32_16x16x32_bf16 v[60:63], v[108:111], v[132:135], v[60:63]\n\t"
      "v_cvt_pk_bf16_f32 v94, v28, v29\n\t"
      "v_cvt_pk_bf16_f32 v95, v30, v31\n\t"
      "s_waitcnt lgkmcnt(0)\n\t"
      "v_mfma_f32_16x16x32_bf16 v[64:67], v[112:115], v[128:131], v[64:67]\n\t"
      "v_cvt_pk_bf16_f32 v96, v32, v33\n\t"
      "v_cvt_pk_bf16_f32 v97, v34, v35\n\t"
      "v_mfma_f32_16x16x32_bf16 v[68:71], v[112:115], v[132:135], v[68:71]\n\t"
      "v_cvt_pk_bf16_f32 v98, v40, v41\n\t"
      "v_cvt_pk_bf16_f32 v99, v42, v43\n\t"
      "v_mfma_f32_16x16x32_bf16 v[72:75], v[116:119], v[128:131], v[72:75]\n\t"
      "v_cvt_pk_bf16_f32 v100, v36, v37\n\t"
      "v_cvt_pk_bf16_f32 v101, v38, v39\n\t"
      "v_mfma_f32_16x16x32_bf16 v[76:79], v[116:119], v[132:135], v[76:79]\n\t"
      "v_cvt_pk_bf16_f32 v102, v44, v45\n\t"
      "v_cvt_pk_bf16_f32 v103, v46, v47\n\t"
      "s_mov_b32 s49, s48\n\t"
      "LQDONE%=:\n\t"
      "s_cmp_eq_u32 s52, 0\n\t"
      "s_cbranch_scc1 LNOQP%=\n\t"
      "v_cvt_pk_bf16_f32 v104, v48, v49\n\t"
      "v_cvt_pk_bf16_f32 v105, v50, v51\n\t"
      "v_cvt_pk_bf16_f32 v106, v56, v57\n\t"
      "v_cvt_pk_bf16_f32 v107, v58, v59\n\t"
      "v_cvt_pk_bf16_f32 v108, v52, v53\n\t"
      "v_cvt_pk_bf16_f32 v109, v54, v55\n\t"
      "v_cvt_pk_bf16_f32 v110, v60, v61\n\t"
      "v_cvt_pk_bf16_f32 v111, v62, v63\n\t"
      "s_nop 0\n\t"
      "v_cvt_pk_bf16_f32 v112, v64, v65\n\t"
      "v_cvt_pk_bf16_f32 v113, v66, v67\n\t"
      "v_cvt_pk_bf16_f32 v114, v72, v73\n\t"
      "v_cvt_pk_bf16_f32 v115, v74, v75\n\t"
      "v_cvt_pk_bf16_f32 v116, v68, v69\n\t"
      "v_cvt_pk_bf16_f32 v117, v70, v71\n\t"
      "v_cvt_pk_bf16_f32 v118, v76, v77\n\t"
      "v_cvt_pk_bf16_f32 v119, v78, v79\n\t"
      "s_waitcnt vmcnt(0)\n\t"
      "v_readfirstlane_b32 s40, v206\n\t"
      "v_readfirstlane_b32 s41, v207\n\t"
      "s_add_u32 s40, s40, 1\n\t"
      "s_add_u32 s41, s41, 1\n\t"
      "s_xor_b32 s40, s40, 0x5bd1e995\n\t"
      "s_xor_b32 s41, s41, 0x9e3779b9\n\t"
      "v_mov_b32 v204, 147456\n\t"
      "v_mov_b32 v206, s40\n\t"
      "v_mov_b32 v207, s41\n\t"
      "ds_write_b64 v204, v[206:207]\n\t"
      "LNOQP%=:\n\t"
      "s_and_b32 s50, s13, 1\n\t"
      "s_cmp_lg_u32 s50, s52\n\t"
      "s_cbranch_scc1 LNOK%=\n\t"
      "s_mul_i32 s53, s48, 36864\n\t"
      "v_add_u32 v244, s53, v11\n\t"
      "s_add_u32 s48, s48, 1\n\t"
      "s_and_b32 s48, s48, 3\n\t"
      "v_xor_b32 v245, 32, v244\n\t"
      "v_xor_b32 v246, 64, v244\n\t"
      "v_xor_b32 v247, 96, v244\n\t"
      "v_xor_b32 v248, 128, v244\n\t"
      "v_xor_b32 v249, 160, v244\n\t"
      "v_xor_b32 v250, 192, v244\n\t"
      "v_xor_b32 v251, 224, v244\n\t"
      "s_waitcnt lgkmcnt(0)\n\t"
      "s_barrier\n\t"
      "ds_read_b64 v[212:213], v244\n\t"
      "ds_read_b64 v[228:229], v244 offset:4096\n\t"
      "ds_read_b64 v[214:215], v245\n\t"
      "ds_read_b64 v[230:231], v245 offset:4096\n\t"
      "ds_read_b64 v[216:217], v246\n\t"
      "ds_read_b64 v[232:233], v246 offset:4096\n\t"
      "ds_read_b64 v[218:219], v247\n\t"
      "ds_read_b64 v[234:235], v247 offset:4096\n\t"
      "ds_read_b64 v[220:221], v248\n\t"
      "ds_read_b64 v[236:237], v248 offset:4096\n\t"
      "ds_read_b64 v[222:223], v249\n\t"
      "ds_read_b64 v[238:239], v249 offset:4096\n\t"
      "ds_read_b64 v[224:225], v250\n\t"
      "ds_read_b64 v[240:241], v250 offset:4096\n\t"
      "ds_read_b64 v[226:227], v251\n\t"
      "ds_read_b64 v[242:243], v251 offset:4096\n\t"
      "LNOK%=:\n\t"
      "s_cmp_eq_u32 s52, 0\n\t"
      "s_cbranch_scc1 LNOSM%=\n\t"
      "s_waitcnt lgkmcnt(12)\n\t"
      "v_mfma_f32_16x16x32_bf16 v[120:123], v[212:215], v[88:91], 0\n\t"
      "v_mfma_f32_16x16x32_bf16 v[128:131], v[228:231], v[88:91], 0\n\t"
      "v_mfma_f32_16x16x32_bf16 v[124:127], v[212:215], v[92:95], 0\n\t"
      "v_mfma_f32_16x16x32_bf16 v[132:135], v[228:231], v[92:95], 0\n\t"
      "s_waitcnt lgkmcnt(8)\n\t"
      "v_mfma_f32_16x16x32_bf16 v[120:123], v[216:219], v[96:99], v[120:123]\n\t"
      "v_mfma_f32_16x16x32_bf16 v[128:131], v[232:235], v[96:99], v[128:131]\n\t"
      "v_mfma_f32_16x16x32_bf16 v[124:127], v[216:219], v[100:103], v[124:127]\n\t"
      "v_mfma_f32_16x16x32_bf16 v[132:135], v[232:235], v[100:103], v[132:135]\n\t"
      "s_waitcnt lgkmcnt(4)\n\t"
      "v_mfma_f32_16x16x32_bf16 v[120:123], v[220:223], v[104:107], v[120:123]\n\t"
      "v_mfma_f32_16x16x32_bf16 v[128:131], v[236:239], v[104:107], v[128:131]\n\t"
      "v_mfma_f32_16x16x32_bf16 v[124:127], v[220:223], v[108:111], v[124:127]\n\t"
      "v_mfma_f32_16x16x32_bf16 v[132:135], v[236:239], v[108:111], v[132:135]\n\t"
      "s_waitcnt lgkmcnt(0)\n\t"
      "v_mfma_f32_16x16x32_bf16 v[120:123], v[224:227], v[112:115], v[120:123]\n\t"
      "v_mfma_f32_16x16x32_bf16 v[128:131], v[240:243], v[112:115], v[128:131]\n\t"
      "v_mfma_f32_16x16x32_bf16 v[124:127], v[224:227], v[116:119], v[124:127]\n\t"
      "v_mfma_f32_16x16x32_bf16 v[132:135], v[240:243], v[116:119], v[132:135]\n\t"
      "s_nop 5\n\t"
      "v_max3_f32 v242, v120, v121, v122\n\t"
      "v_max3_f32 v243, v123, v128, v129\n\t"
      "s_nop 0\n\t"
      "v_max3_f32 v244, v124, v125, v126\n\t"
      "v_max3_f32 v245, v127, v132, v133\n\t"
      "v_max3_f32 v242, v242, v130, v131\n\t"
      "v_max3_f32 v244, v244, v134, v135\n\t"
      "v_max_f32 v242, v242, v243\n\t"
      "v_max_f32 v244, v244, v245\n\t"
      "v_mov_b32 v243, v242\n\t"
      "v_mov_b32 v245, v244\n\t"
      "s_nop 1\n\t"
      "v_permlane16_swap_b32 v242, v243\n\t"
      "v_permlane16_swap_b32 v244, v245\n\t"
      "v_max_f32 v242, v242, v243\n\t"
      "v_max_f32 v244, v244, v245\n\t"
      "v_mov_b32 v243, v242\n\t"
      "v_mov_b32 v245, v244\n\t"
      "s_nop 1\n\t"
      "v_permlane32_swap_b32 v242, v243\n\t"
      "v_permlane32_swap_b32 v244, v245\n\t"
      "v_max_f32 v242, v242, v243\n\t"
      "v_max_f32 v244, v244, v245\n\t"
      "v_mul_f32 v242, s55, v242\n\t"
      "v_mul_f32 v244, s55, v244\n\t"
      "v_pk_fma_f32 v[120:121], v[120:121], s[54:55], v[242:243] op_sel:[0,1,0] op_sel_hi:[1,1,0] neg_lo:[0,0,1] neg_hi:[0,0,1]\n\t"
      "v_pk_fma_f32 v[122:123], v[122:123], s[54:55], v[242:243] op_sel:[0,1,0] op_sel_hi:[1,1,0] neg_lo:[0,0,1] neg_hi:[0,0,1]\n\t"
      "v_pk_fma_f32 v[128:129], v[128:129], s[54:55], v[242:243] op_sel:[0,1,0] op_sel_hi:[1,1,0] neg_lo:[0,0,1] neg_hi:[0,0,1]\n\t"
      "v_pk_fma_f32 v[130:131], v[130:131], s[54:55], v[242:243] op_sel:[0,1,0] op_sel_hi:[1,1,0] neg_lo:[0,0,1] neg_hi:[0,0,1]\n\t"
      "v_pk_fma_f32 v[124:125], v[124:125], s[54:55], v[244:245] op_sel:[0,1,0] op_sel_hi:[1,1,0] neg_lo:[0,0,1] neg_hi:[0,0,1]\n\t"
      "v_pk_fma_f32 v[126:127], v[126:127], s[54:55], v[244:245] op_sel:[0,1,0] op_sel_hi:[1,1,0] neg_lo:[0,0,1] neg_hi:[0,0,1]\n\t"
      "v_pk_fma_f32 v[132:133], v[132:133], s[54:55], v[244:245] op_sel:[0,1,0] op_sel_hi:[1,1,0] neg_lo:[0,0,1] neg_hi:[0,0,1]\n\t"
      "v_pk_fma_f32 v[134:135], v[134:135], s[54:55], v[244:245] op_sel:[0,1,0] op_sel_hi:[1,1,0] neg_lo:[0,0,1] neg_hi:[0,0,1]\n\t"
      "v_exp_f32 v120, v120\n\t"
      "v_exp_f32 v121, v121\n\t"
      "v_exp_f32 v122, v122\n\t"
      "v_exp_f32 v123, v123\n\t"
      "v_exp_f32 v128, v128\n\t"
      "v_exp_f32 v129, v129\n\t"
      "v_exp_f32 v130, v130\n\t"
      "v_exp_f32 v131, v131\n\t"
      "v_exp_f32 v124, v124\n\t"
      "v_exp_f32 v125, v125\n\t"
      "v_exp_f32 v126, v126\n\t"
      "v_exp_f32 v127, v127\n\t"
      "v_exp_f32 v132, v132\n\t"
      "v_exp_f32 v133, v133\n\t"
      "v_exp_f32 v134, v134\n\t"
      "v_exp_f32 v135, v135\n\t"
      "s_nop 0\n\t"
      "v_pk_add_f32 v[246:247], v[120:121], v[122:123]\n\t"
      "v_pk_add_f32 v[250:251], v[128:129], v[130:131]\n\t"
      "v_pk_add_f32 v[248:249], v[124:125], v[126:127]\n\t"
      "v_pk_add_f32 v[252:253], v[132:133], v[134:135]\n\t"
      "v_pk_add_f32 v[246:247], v[246:247], v[250:251]\n\t"
      "v_pk_add_f32 v[248:249], v[248:249], v[252:253]\n\t"
      "v_add_f32 v242, v246, v247\n\t"
      "v_add_f32 v244, v248, v249\n\t"
      "v_mov_b32 v243, v242\n\t"
      "v_mov_b32 v245, v244\n\t"
      "s_nop 1\n\t"
      "v_permlane16_swap_b32 v242, v243\n\t"
      "v_permlane16_swap_b32 v244, v245\n\t"
      "v_add_f32 v242, v242, v243\n\t"
      "v_add_f32 v244, v244, v245\n\t"
      "v_mov_b32 v243, v242\n\t"
      "v_mov_b32 v245, v244\n\t"
      "s_nop 1\n\t"
      "v_permlane32_swap_b32 v242, v243\n\t"
      "v_permlane32_swap_b32 v244, v245\n\t"
      "v_add_f32 v242, v242, v243\n\t"
      "v_add_f32 v244, v244, v245\n\t"
      "v_rcp_f32 v246, v242\n\t"
      "v_rcp_f32 v248, v244\n\t"
      "s_nop 0\n\t"
      "v_pk_mul_f32 v[120:121], v[120:121], v[246:247] op_sel_hi:[1,0]\n\t"
      "v_pk_mul_f32 v[122:123], v[122:123], v[246:247] op_sel_hi:[1,0]\n\t"
      "v_pk_mul_f32 v[128:129], v[128:129], v[246:247] op_sel_hi:[1,0]\n\t"
      "v_pk_mul_f32 v[130:131], v[130:131], v[246:247] op_sel_hi:[1,0]\n\t"
      "v_cvt_pk_bf16_f32 v204, v120, v121\n\t"
      "v_cvt_pk_bf16_f32 v205, v122, v123\n\t"
      "v_cvt_pk_bf16_f32 v206, v128, v129\n\t"
      "v_cvt_pk_bf16_f32 v207, v130, v131\n\t"
      "buffer_store_dwordx4 v[204:207], v13, s[44:47], s58 offen offset:0 sc1\n\t"
      "v_pk_mul_f32 v[124:125], v[124:125], v[248:249] op_sel_hi:[1,0]\n\t"
      "v_pk_mul_f32 v[126:127], v[126:127], v[248:249] op_sel_hi:[1,0]\n\t"
      "v_pk_mul_f32 v[132:133], v[132:133], v[248:249] op_sel_hi:[1,0]\n\t"
      "v_pk_mul_f32 v[134:135], v[134:135], v[248:249] op_sel_hi:[1,0]\n\t"
      "v_cvt_pk_bf16_f32 v208, v124, v125\n\t"
      "v_cvt_pk_bf16_f32 v209, v126, v127\n\t"
      "v_cvt_pk_bf16_f32 v210, v132, v133\n\t"
      "v_cvt_pk_bf16_f32 v211, v134, v135\n\t"
      "buffer_store_dwordx4 v[208:211], v13, s[44:47], s58 offen offset:1024 sc1\n\t"
      "v_add3_u32 v242, v204, v206, v208\n\t"
      "v_add3_u32 v243, v205, v207, v209\n\t"
      "v_add3_u32 v242, v242, v210, s40\n\t"
      "v_add3_u32 v243, v243, v211, s41\n\t"
      "buffer_store_dwordx2 v[242:243], v14, s[44:47], s58 offen offset:2048 sc1\n\t"
      "s_barrier\n\t"
      "LNH%=:\n\t"
      "LNOSM%=:\n\t"
      "s_bfe_u32 s50, s13, 0x10001\n\t"
      "s_cmp_lg_u32 s50, s52\n\t"
      "s_cbranch_scc1 LNOV0%=\n\t"
      "s_mul_i32 s53, s48, 36864\n\t"
      "v_add_u32 v244, s53, v11\n\t"
      "s_add_u32 s48, s48, 1\n\t"
      "s_and_b32 s48, s48, 3\n\t"
      "v_xor_b32 v245, 32, v244\n\t"
      "v_xor_b32 v246, 64, v244\n\t"
      "v_xor_b32 v247, 96, v244\n\t"
      "v_xor_b32 v248, 128, v244\n\t"
      "v_xor_b32 v249, 160, v244\n\t"
      "v_xor_b32 v250, 192, v244\n\t"
      "v_xor_b32 v251, 224, v244\n\t"
      "s_waitcnt lgkmcnt(0)\n\t"
      "s_barrier\n\t"
      "ds_read_b64 v[136:137], v244\n\t"
      "ds_read_b64 v[152:153], v244 offset:4096\n\t"
      "ds_read_b64 v[138:139], v245\n\t"
      "ds_read_b64 v[154:155], v245 offset:4096\n\t"
      "ds_read_b64 v[140:141], v246\n\t"
      "ds_read_b64 v[156:157], v246 offset:4096\n\t"
      "ds_read_b64 v[142:143], v247\n\t"
      "ds_read_b64 v[158:159], v247 offset:4096\n\t"
      "ds_read_b64 v[144:145], v248\n\t"
      "ds_read_b64 v[160:161], v248 offset:4096\n\t"
      "ds_read_b64 v[146:147], v249\n\t"
      "ds_read_b64 v[162:163], v249 offset:4096\n\t"
      "ds_read_b64 v[148:149], v250\n\t"
      "ds_read_b64 v[164:165], v250 offset:4096\n\t"
      "ds_read_b64 v[150:151], v251\n\t"
      "ds_read_b64 v[166:167], v251 offset:4096\n\t"
      "LNOV0%=:\n\t"
      "s_lshr_b32 s50, s13, 2\n\t"
      "s_cmp_lg_u32 s50, s52\n\t"
      "s_cbranch_scc1 LNOV%=\n\t"
      "s_mul_i32 s53, s48, 36864\n\t"
      "v_add_u32 v15, s53, v12\n\t"
      "s_add_u32 s48, s48, 1\n\t"
      "s_and_b32 s48, s48, 3\n\t"
      "s_waitcnt lgkmcnt(0)\n\t"
      "s_barrier\n\t"
      "ds_read_b64_tr_b16 v[88:89], v15 offset:0\n\t"
      "ds_read_b64_tr_b16 v[90:91], v15 offset:4608\n\t"
      "ds_read_b64_tr_b16 v[92:93], v15 offset:9216\n\t"
      "ds_read_b64_tr_b16 v[94:95], v15 offset:13824\n\t"
      "ds_read_b64_tr_b16 v[96:97], v15 offset:18432\n\t"
      "ds_read_b64_tr_b16 v[98:99], v15 offset:23040\n\t"
      "ds_read_b64_tr_b16 v[100:101], v15 offset:27648\n\t"
      "ds_read_b64_tr_b16 v[102:103], v15 offset:32256\n\t"
      "ds_read_b64_tr_b16 v[104:105], v15 offset:32\n\t"
      "ds_read_b64_tr_b16 v[106:107], v15 offset:4640\n\t"
      "ds_read_b64_tr_b16 v[108:109], v15 offset:9248\n\t"
      "ds_read_b64_tr_b16 v[110:111], v15 offset:13856\n\t"
      "s_waitcnt lgkmcnt(8)\n\t"
      "v_mfma_f32_16x16x32_bf16 v[120:123], v[136:139], v[88:91], 0\n\t"
      "ds_read_b64_tr_b16 v[112:113], v15 offset:18464\n\t"
      "v_mfma_f32_16x16x32_bf16 v[124:127], v[152:155], v[88:91], 0\n\t"
      "ds_read_b64_tr_b16 v[114:115], v15 offset:23072\n\t"
      "v_mfma_f32_16x16x32_bf16 v[120:123], v[140:143], v[92:95], v[120:123]\n\t"
      "ds_read_b64_tr_b16 v[116:117], v15 offset:27680\n\t"
      "v_mfma_f32_16x16x32_bf16 v[124:127], v[156:159], v[92:95], v[124:127]\n\t"
      "ds_read_b64_tr_b16 v[118:119], v15 offset:32288\n\t"
      "s_waitcnt lgkmcnt(8)\n\t"
      "v_mfma_f32_16x16x32_bf16 v[120:123], v[144:147], v[96:99], v[120:123]\n\t"
      "ds_read_b64_tr_b16 v[88:89], v15 offset:64\n\t"
      "v_mfma_f32_16x16x32_bf16 v[124:127], v[160:163], v[96:99], v[124:127]\n\t"
      "ds_read_b64_tr_b16 v[90:91], v15 offset:4672\n\t"
      "v_mfma_f32_16x16x32_bf16 v[120:123], v[148:151], v[100:103], v[120:123]\n\t"
      "ds_read_b64_tr_b16 v[92:93], v15 offset:9280\n\t"
      "v_mfma_f32_16x16x32_bf16 v[124:127], v[164:167], v[100:103], v[124:127]\n\t"
      "ds_read_b64_tr_b16 v[94:95], v15 offset:13888\n\t"
      "s_waitcnt lgkmcnt(8)\n\t"
      "v_mfma_f32_16x16x32_bf16 v[128:131], v[136:139], v[104:107], 0\n\t"
      "ds_read_b64_tr_b16 v[96:97], v15 offset:18496\n\t"
      "v_mfma_f32_16x16x32_bf16 v[132:135], v[152:155], v[104:107], 0\n\t"
      "ds_read_b64_tr_b16 v[98:99], v15 offset:23104\n\t"
      "v_mfma_f32_16x16x32_bf16 v[128:131], v[140:143], v[108:111], v[128:131]\n\t"
      "ds_read_b64_tr_b16 v[100:101], v15 offset:27712\n\t"
      "v_mfma_f32_16x16x32_bf16 v[132:135], v[156:159], v[108:111], v[132:135]\n\t"
      "ds_read_b64_tr_b16 v[102:103], v15 offset:32320\n\t"
      "s_waitcnt lgkmcnt(8)\n\t"
      "v_mfma_f32_16x16x32_bf16 v[128:131], v[144:147], v[112:115], v[128:131]\n\t"
      "ds_read_b64_tr_b16 v[104:105], v15 offset:96\n\t"
      "v_cvt_pk_bf16_f32 v168, v120, v121\n\t"
      "v_mfma_f32_16x16x32_bf16 v[132:135], v[160:163], v[112:115], v[132:135]\n\t"
      "ds_read_b64_tr_b16 v[106:107], v15 offset:4704\n\t"
      "v_cvt_pk_bf16_f32 v169, v122, v123\n\t"
      "v_mfma_f32_16x16x32_bf16 v[128:131], v[148:151], v[116:119], v[128:131]\n\t"
      "ds_read_b64_tr_b16 v[108:109], v15 offset:9312\n\t"
      "v_cvt_pk_bf16_f32 v170, v124, v125\n\t"
      "v_mfma_f32_16x16x32_bf16 v[132:135], v[164:167], v[116:119], v[132:135]\n\t"
      "ds_read_b64_tr_b16 v[110:111], v15 offset:13920\n\t"
      "v_cvt_pk_bf16_f32 v171, v126, v127\n\t"
      "s_waitcnt lgkmcnt(8)\n\t"
      "v_mfma_f32_16x16x32_bf16 v[120:123], v[136:139], v[88:91], 0\n\t"
      "ds_read_b64_tr_b16 v[112:113], v15 offset:18528\n\t"
      "v_mfma_f32_16x16x32_bf16 v[124:127], v[152:155], v[88:91], 0\n\t"
      "ds_read_b64_tr_b16 v[114:115], v15 offset:23136\n\t"
      "v_mfma_f32_16x16x32_bf16 v[120:123], v[140:143], v[92:95], v[120:123]\n\t"
      "ds_read_b64_tr_b16 v[116:117], v15 offset:27744\n\t"
      "v_mfma_f32_16x16x32_bf16 v[124:127], v[156:159], v[92:95], v[124:127]\n\t"
      "ds_read_b64_tr_b16 v[118:119], v15 offset:32352\n\t"
      "s_waitcnt lgkmcnt(8)\n\t"
      "v_mfma_f32_16x16x32_bf16 v[120:123], v[144:147], v[96:99], v[120:123]\n\t"
      "ds_read_b64_tr_b16 v[88:89], v15 offset:128\n\t"
      "v_cvt_pk_bf16_f32 v172, v128, v129\n\t"
      "v_mfma_f32_16x16x32_bf16 v[124:127], v[160:163], v[96:99], v[124:127]\n\t"
      "ds_read_b64_tr_b16 v[90:91], v15 offset:4736\n\t"
      "v_cvt_pk_bf16_f32 v173, v130, v131\n\t"
      "v_mfma_f32_16x16x32_bf16 v[120:123], v[148:151], v[100:103], v[120:123]\n\t"
      "ds_read_b64_tr_b16 v[92:93], v15 offset:9344\n\t"
      "v_cvt_pk_bf16_f32 v174, v132, v133\n\t"
      "v_mfma_f32_16x16x32_bf16 v[124:127], v[164:167], v[100:103], v[124:127]\n\t"
      "ds_read_b64_tr_b16 v[94:95], v15 offset:13952\n\t"
      "v_cvt_pk_bf16_f32 v175, v134, v135\n\t"
      "s_waitcnt lgkmcnt(8)\n\t"
      "v_mfma_f32_16x16x32_bf16 v[128:131], v[136:139], v[104:107], 0\n\t"
      "ds_read_b64_tr_b16 v[96:97], v15 offset:18560\n\t"
      "v_mfma_f32_16x16x32_bf16 v[132:135], v[152:155], v[104:107], 0\n\t"
      "ds_read_b64_tr_b16 v[98:99], v15 offset:23168\n\t"
      "v_mfma_f32_16x16x32_bf16 v[128:131], v[140:143], v[108:111], v[128:131]\n\t"
      "ds_read_b64_tr_b16 v[100:101], v15 offset:27776\n\t"
      "v_mfma_f32_16x16x32_bf16 v[132:135], v[156:159], v[108:111], v[132:135]\n\t"
      "ds_read_b64_tr_b16 v[102:103], v15 offset:32384\n\t"
      "s_waitcnt lgkmcnt(8)\n\t"
      "v_mfma_f32_16x16x32_bf16 v[128:131], v[144:147], v[112:115], v[128:131]\n\t"
      "ds_read_b64_tr_b16 v[104:105], v15 offset:160\n\t"
      "v_cvt_pk_bf16_f32 v176, v120, v121\n\t"
      "v_mfma_f32_16x16x32_bf16 v[132:135], v[160:163], v[112:115], v[132:135]\n\t"
      "ds_read_b64_tr_b16 v[106:107], v15 offset:4768\n\t"
      "v_cvt_pk_bf16_f32 v177, v122, v123\n\t"
      "v_mfma_f32_16x16x32_bf16 v[128:131], v[148:151], v[116:119], v[128:131]\n\t"
      "ds_read_b64_tr_b16 v[108:109], v15 offset:9376\n\t"
      "v_cvt_pk_bf16_f32 v178, v124, v125\n\t"
      "v_mfma_f32_16x16x32_bf16 v[132:135], v[164:167], v[116:119], v[132:135]\n\t"
      "ds_read_b64_tr_b16 v[110:111], v15 offset:13984\n\t"
      "v_cvt_pk_bf16_f32 v179, v126, v127\n\t"
      "s_waitcnt lgkmcnt(8)\n\t"
      "v_mfma_f32_16x16x32_bf16 v[120:123], v[136:139], v[88:91], 0\n\t"
      "ds_read_b64_tr_b16 v[112:113], v15 offset:18592\n\t"
      "v_mfma_f32_16x16x32_bf16 v[124:127], v[152:155], v[88:91], 0\n\t"
      "ds_read_b64_tr_b16 v[114:115], v15 offset:23200\n\t"
      "v_mfma_f32_16x16x32_bf16 v[120:123], v[140:143], v[92:95], v[120:123]\n\t"
      "ds_read_b64_tr_b16 v[116:117], v15 offset:27808\n\t"
      "v_mfma_f32_16x16x32_bf16 v[124:127], v[156:159], v[92:95], v[124:127]\n\t"
      "ds_read_b64_tr_b16 v[118:119], v15 offset:32416\n\t"
      "s_waitcnt lgkmcnt(8)\n\t"
      "v_mfma_f32_16x16x32_bf16 v[120:123], v[144:147], v[96:99], v[120:123]\n\t"
      "ds_read_b64_tr_b16 v[88:89], v15 offset:192\n\t"
      "v_cvt_pk_bf16_f32 v180, v128, v129\n\t"
      "v_mfma_f32_16x16x32_bf16 v[124:127], v[160:163], v[96:99], v[124:127]\n\t"
      "ds_read_b64_tr_b16 v[90:91], v15 offset:4800\n\t"
      "v_cvt_pk_bf16_f32 v181, v130, v131\n\t"
      "v_mfma_f32_16x16x32_bf16 v[120:123], v[148:151], v[100:103], v[120:123]\n\t"
      "ds_read_b64_tr_b16 v[92:93], v15 offset:9408\n\t"
      "v_cvt_pk_bf16_f32 v182, v132, v133\n\t"
      "v_mfma_f32_16x16x32_bf16 v[124:127], v[164:167], v[100:103], v[124:127]\n\t"
      "ds_read_b64_tr_b16 v[94:95], v15 offset:14016\n\t"
      "v_cvt_pk_bf16_f32 v183, v134, v135\n\t"
      "s_waitcnt lgkmcnt(8)\n\t"
      "v_mfma_f32_16x16x32_bf16 v[128:131], v[136:139], v[104:107], 0\n\t"
      "ds_read_b64_tr_b16 v[96:97], v15 offset:18624\n\t"
      "v_mfma_f32_16x16x32_bf16 v[132:135], v[152:155], v[104:107], 0\n\t"
      "ds_read_b64_tr_b16 v[98:99], v15 offset:23232\n\t"
      "v_mfma_f32_16x16x32_bf16 v[128:131], v[140:143], v[108:111], v[128:131]\n\t"
      "ds_read_b64_tr_b16 v[100:101], v15 offset:27840\n\t"
      "v_mfma_f32_16x16x32_bf16 v[132:135], v[156:159], v[108:111], v[132:135]\n\t"
      "ds_read_b64_tr_b16 v[102:103], v15 offset:32448\n\t"
      "s_waitcnt lgkmcnt(8)\n\t"
      "v_mfma_f32_16x16x32_bf16 v[128:131], v[144:147], v[112:115], v[128:131]\n\t"
      "ds_read_b64_tr_b16 v[104:105], v15 offset:224\n\t"
      "v_cvt_pk_bf16_f32 v184, v120, v121\n\t"
      "v_mfma_f32_16x16x32_bf16 v[132:135], v[160:163], v[112:115], v[132:135]\n\t"
      "ds_read_b64_tr_b16 v[106:107], v15 offset:4832\n\t"
      "v_cvt_pk_bf16_f32 v185, v122, v123\n\t"
      "v_mfma_f32_16x16x32_bf16 v[128:131], v[148:151], v[116:119], v[128:131]\n\t"
      "ds_read_b64_tr_b16 v[108:109], v15 offset:9440\n\t"
      "v_cvt_pk_bf16_f32 v186, v124, v125\n\t"
      "v_mfma_f32_16x16x32_bf16 v[132:135], v[164:167], v[116:119], v[132:135]\n\t"
      "ds_read_b64_tr_b16 v[110:111], v15 offset:14048\n\t"
      "v_cvt_pk_bf16_f32 v187, v126, v127\n\t"
      "s_waitcnt lgkmcnt(8)\n\t"
      "v_mfma_f32_16x16x32_bf16 v[120:123], v[136:139], v[88:91], 0\n\t"
      "ds_read_b64_tr_b16 v[112:113], v15 offset:18656\n\t"
      "v_mfma_f32_16x16x32_bf16 v[124:127], v[152:155], v[88:91], 0\n\t"
      "ds_read_b64_tr_b16 v[114:115], v15 offset:23264\n\t"
      "v_mfma_f32_16x16x32_bf16 v[120:123], v[140:143], v[92:95], v[120:123]\n\t"
      "ds_read_b64_tr_b16 v[116:117], v15 offset:27872\n\t"
      "v_mfma_f32_16x16x32_bf16 v[124:127], v[156:159], v[92:95], v[124:127]\n\t"
      "ds_read_b64_tr_b16 v[118:119], v15 offset:32480\n\t"
      "s_waitcnt lgkmcnt(8)\n\t"
      "v_mfma_f32_16x16x32_bf16 v[120:123], v[144:147], v[96:99], v[120:123]\n\t"
      "ds_read_b64_tr_b16 v[88:89], v15 offset:256\n\t"
      "v_cvt_pk_bf16_f32 v188, v128, v129\n\t"
      "v_mfma_f32_16x16x32_bf16 v[124:127], v[160:163], v[96:99], v[124:127]\n\t"
      "ds_read_b64_tr_b16 v[90:91], v15 offset:4864\n\t"
      "v_cvt_pk_bf16_f32 v189, v130, v131\n\t"
      "v_mfma_f32_16x16x32_bf16 v[120:123], v[148:151], v[100:103], v[120:123]\n\t"
      "ds_read_b64_tr_b16 v[92:93], v15 offset:9472\n\t"
      "v_cvt_pk_bf16_f32 v190, v132, v133\n\t"
      "v_mfma_f32_16x16x32_bf16 v[124:127], v[164:167], v[100:103], v[124:127]\n\t"
      "ds_read_b64_tr_b16 v[94:95], v15 offset:14080\n\t"
      "v_cvt_pk_bf16_f32 v191, v134, v135\n\t"
      "s_waitcnt lgkmcnt(8)\n\t"
      "v_mfma_f32_16x16x32_bf16 v[128:131], v[136:139], v[104:107], 0\n\t"
      "ds_read_b64_tr_b16 v[96:97], v15 offset:18688\n\t"
      "v_mfma_f32_16x16x32_bf16 v[132:135], v[152:155], v[104:107], 0\n\t"
      "ds_read_b64_tr_b16 v[98:99], v15 offset:23296\n\t"
      "v_mfma_f32_16x16x32_bf16 v[128:131], v[140:143], v[108:111], v[128:131]\n\t"
      "ds_read_b64_tr_b16 v[100:101], v15 offset:27904\n\t"
      "v_mfma_f32_16x16x32_bf16 v[132:135], v[156:159], v[108:111], v[132:135]\n\t"
      "ds_read_b64_tr_b16 v[102:103], v15 offset:32512\n\t"
      "s_waitcnt lgkmcnt(8)\n\t"
      "v_mfma_f32_16x16x32_bf16 v[128:131], v[144:147], v[112:115], v[128:131]\n\t"
      "v_cvt_pk_bf16_f32 v192, v120, v121\n\t"
      "v_mfma_f32_16x16x32_bf16 v[132:135], v[160:163], v[112:115], v[132:135]\n\t"
      "v_cvt_pk_bf16_f32 v193, v122, v123\n\t"
      "v_mfma_f32_16x16x32_bf16 v[128:131], v[148:151], v[116:119], v[128:131]\n\t"
      "v_cvt_pk_bf16_f32 v194, v124, v125\n\t"
      "v_mfma_f32_16x16x32_bf16 v[132:135], v[164:167], v[116:119], v[132:135]\n\t"
      "v_cvt_pk_bf16_f32 v195, v126, v127\n\t"
      "s_waitcnt lgkmcnt(4)\n\t"
      "v_mfma_f32_16x16x32_bf16 v[120:123], v[136:139], v[88:91], 0\n\t"
      "v_mfma_f32_16x16x32_bf16 v[124:127], v[152:155], v[88:91], 0\n\t"
      "v_mfma_f32_16x16x32_bf16 v[120:123], v[140:143], v[92:95], v[120:123]\n\t"
      "v_mfma_f32_16x16x32_bf16 v[124:127], v[156:159], v[92:95], v[124:127]\n\t"
      "s_waitcnt lgkmcnt(0)\n\t"
      "v_mfma_f32_16x16x32_bf16 v[120:123], v[144:147], v[96:99], v[120:123]\n\t"
      "v_cvt_pk_bf16_f32 v196, v128, v129\n\t"
      "v_mfma_f32_16x16x32_bf16 v[124:127], v[160:163], v[96:99], v[124:127]\n\t"
      "v_cvt_pk_bf16_f32 v197, v130, v131\n\t"
      "v_mfma_f32_16x16x32_bf16 v[120:123], v[148:151], v[100:103], v[120:123]\n\t"
      "v_cvt_pk_bf16_f32 v198, v132, v133\n\t"
      "v_mfma_f32_16x16x32_bf16 v[124:127], v[164:167], v[100:103], v[124:127]\n\t"
      "v_cvt_pk_bf16_f32 v199, v134, v135\n\t"
      "s_nop 7\n\t"
      "v_cvt_pk_bf16_f32 v200, v120, v121\n\t"
      "v_cvt_pk_bf16_f32 v201, v122, v123\n\t"
      "v_cvt_pk_bf16_f32 v202, v124, v125\n\t"
      "v_cvt_pk_bf16_f32 v203, v126, v127\n\t"
      "LNOV%=:\n\t"
      "s_cmp_lg_u32 s52, 0\n\t"
      "s_cbranch_scc1 LSEGDONE%=\n\t"
      "s_mov_b32 s52, 1\n\t"
      "s_sub_u32 s51, 9, s12\n\t"
      "s_branch LSEG%=\n\t"
      "LSEGDONE%=:\n\t"
      "s_mul_i32 s53, s48, 36864\n\t"
      "v_add_u32 v2, s53, v11\n\t"
      "v_xor_b32 v3, 32, v2\n\t"
      "v_xor_b32 v4, 64, v2\n\t"
      "v_xor_b32 v5, 96, v2\n\t"
      "v_xor_b32 v6, 128, v2\n\t"
      "v_xor_b32 v7, 160, v2\n\t"
      "v_xor_b32 v8, 192, v2\n\t"
      "v_xor_b32 v9, 224, v2\n\t"
      "s_add_u32 s48, s48, 1\n\t"
      "s_and_b32 s48, s48, 3\n\t"
      "s_mul_i32 s53, s48, 36864\n\t"
      "v_add_u32 v15, s53, v12\n\t"
      "s_add_u32 s48, s48, 1\n\t"
      "s_and_b32 s48, s48, 3\n\t"
      "LNOPG%=:\n\t"
      "s_barrier\n\t"
      "ds_read_b64 v[136:137], v2\n\t"
      "ds_read_b64 v[152:153], v2 offset:4096\n\t"
      "ds_read_b64 v[138:139], v3\n\t"
      "ds_read_b64 v[154:155], v3 offset:4096\n\t"
      "ds_read_b64 v[140:141], v4\n\t"
      "ds_read_b64 v[156:157], v4 offset:4096\n\t"
      "ds_read_b64 v[142:143], v5\n\t"
      "ds_read_b64 v[158:159], v5 offset:4096\n\t"
      "ds_read_b64_tr_b16 v[88:89], v15 offset:0\n\t"
      "ds_read_b64_tr_b16 v[90:91], v15 offset:4608\n\t"
      "ds_read_b64_tr_b16 v[92:93], v15 offset:9216\n\t"
      "ds_read_b64_tr_b16 v[94:95], v15 offset:13824\n\t"
      "v_mfma_f32_16x16x32_bf16 v[16:19], v[168:171], v[204:207], 0\n\t"
      "ds_read_b64 v[144:145], v6\n\t"
      "ds_read_b64 v[160:161], v6 offset:4096\n\t"
      "ds_read_b64 v[146:147], v7\n\t"
      "v_mfma_f32_16x16x32_bf16 v[20:23], v[168:171], v[208:211], 0\n\t"
      "ds_read_b64 v[162:163], v7 offset:4096\n\t"
      "ds_read_b64 v[148:149], v8\n\t"
      "ds_read_b64 v[164:165], v8 offset:4096\n\t"
      "v_mfma_f32_16x16x32_bf16 v[24:27], v[172:175], v[204:207], 0\n\t"
      "ds_read_b64 v[150:151], v9\n\t"
      "ds_read_b64 v[166:167], v9 offset:4096\n\t"
      "ds_read_b64_tr_b16 v[96:97], v15 offset:18432\n\t"
      "v_mfma_f32_16x16x32_bf16 v[28:31], v[172:175], v[208:211], 0\n\t"
      "ds_read_b64_tr_b16 v[98:99], v15 offset:23040\n\t"
      "ds_read_b64_tr_b16 v[100:101], v15 offset:27648\n\t"
      "ds_read_b64_tr_b16 v[102:103], v15 offset:32256\n\t"
      "v_mfma_f32_16x16x32_bf16 v[32:35], v[176:179], v[204:207], 0\n\t"
      "ds_read_b64_tr_b16 v[104:105], v15 offset:32\n\t"
      "ds_read_b64_tr_b16 v[106:107], v15 offset:4640\n\t"
      "v_mfma_f32_16x16x32_bf16 v[36:39], v[176:179], v[208:211], 0\n\t"
      "ds_read_b64_tr_b16 v[108:109], v15 offset:9248\n\t"
      "ds_read_b64_tr_b16 v[110:111], v15 offset:13856\n\t"
      "s_waitcnt lgkmcnt(15)\n\t"
      "v_mfma_f32_16x16x32_bf16 v[120:123], v[136:139], v[88:91], 0\n\t"
      "ds_read_b64_tr_b16 v[112:113], v15 offset:18464\n\t"
      "v_mfma_f32_16x16x32_bf16 v[124:127], v[152:155], v[88:91], 0\n\t"
      "ds_read_b64_tr_b16 v[114:115], v15 offset:23072\n\t"
      "v_mfma_f32_16x16x32_bf16 v[120:123], v[140:143], v[92:95], v[120:123]\n\t"
      "ds_read_b64_tr_b16 v[116:117], v15 offset:27680\n\t"
      "v_mfma_f32_16x16x32_bf16 v[124:127], v[156:159], v[92:95], v[124:127]\n\t"
      "ds_read_b64_tr_b16 v[118:119], v15 offset:32288\n\t"
      "v_mfma_f32_16x16x32_bf16 v[40:43], v[180:183], v[204:207], 0\n\t"
      "v_mfma_f32_16x16x32_bf16 v[44:47], v[180:183], v[208:211], 0\n\t"
      "s_waitcnt lgkmcnt(8)\n\t"
      "v_mfma_f32_16x16x32_bf16 v[120:123], v[144:147], v[96:99], v[120:123]\n\t"
      "ds_read_b64_tr_b16 v[88:89], v15 offset:64\n\t"
      "v_mfma_f32_16x16x32_bf16 v[124:127], v[160:163], v[96:99], v[124:127]\n\t"
      "ds_read_b64_tr_b16 v[90:91], v15 offset:4672\n\t"
      "v_mfma_f32_16x16x32_bf16 v[120:123], v[148:151], v[100:103], v[120:123]\n\t"
      "ds_read_b64_tr_b16 v[92:93], v15 offset:9280\n\t"
      "v_mfma_f32_16x16x32_bf16 v[124:127], v[164:167], v[100:103], v[124:127]\n\t"
      "ds_read_b64_tr_b16 v[94:95], v15 offset:13888\n\t"
      "s_waitcnt lgkmcnt(8)\n\t"
      "v_mfma_f32_16x16x32_bf16 v[128:131], v[136:139], v[104:107], 0\n\t"
      "ds_read_b64_tr_b16 v[96:97], v15 offset:18496\n\t"
      "s_mul_i32 s53, s48, 36864\n\t"
      "v_mfma_f32_16x16x32_bf16 v[132:135], v[152:155], v[104:107], 0\n\t"
      "ds_read_b64_tr_b16 v[98:99], v15 offset:23104\n\t"
      "v_add_u32 v2, s53, v11\n\t"
      "v_mfma_f32_16x16x32_bf16 v[128:131], v[140:143], v[108:111], v[128:131]\n\t"
      "ds_read_b64_tr_b16 v[100:101], v15 offset:27712\n\t"
      "v_xor_b32 v3, 32, v2\n\t"
      "v_mfma_f32_16x16x32_bf16 v[132:135], v[156:159], v[108:111], v[132:135]\n\t"
      "ds_read_b64_tr_b16 v[102:103], v15 offset:32320\n\t"
      "v_mfma_f32_16x16x32_bf16 v[48:51], v[184:187], v[204:207], 0\n\t"
      "v_xor_b32 v4, 64, v2\n\t"
      "v_xor_b32 v5, 96, v2\n\t"
      "v_mfma_f32_16x16x32_bf16 v[52:55], v[184:187], v[208:211], 0\n\t"
      "s_waitcnt lgkmcnt(8)\n\t"
      "v_mfma_f32_16x16x32_bf16 v[128:131], v[144:147], v[112:115], v[128:131]\n\t"
      "ds_read_b64_tr_b16 v[104:105], v15 offset:96\n\t"
      "v_cvt_pk_bf16_f32 v168, v120, v121\n\t"
      "v_mfma_f32_16x16x32_bf16 v[132:135], v[160:163], v[112:115], v[132:135]\n\t"
      "ds_read_b64_tr_b16 v[106:107], v15 offset:4704\n\t"
      "v_cvt_pk_bf16_f32 v169, v122, v123\n\t"
      "v_mfma_f32_16x16x32_bf16 v[128:131], v[148:151], v[116:119], v[128:131]\n\t"
      "ds_read_b64_tr_b16 v[108:109], v15 offset:9312\n\t"
      "v_cvt_pk_bf16_f32 v170, v124, v125\n\t"
      "v_mfma_f32_16x16x32_bf16 v[132:135], v[164:167], v[116:119], v[132:135]\n\t"
      "ds_read_b64_tr_b16 v[110:111], v15 offset:13920\n\t"
      "v_cvt_pk_bf16_f32 v171, v126, v127\n\t"
      "s_waitcnt lgkmcnt(8)\n\t"
      "v_mfma_f32_16x16x32_bf16 v[120:123], v[136:139], v[88:91], 0\n\t"
      "ds_read_b64_tr_b16 v[112:113], v15 offset:18528\n\t"
      "v_xor_b32 v6, 128, v2\n\t"
      "v_mfma_f32_16x16x32_bf16 v[124:127], v[152:155], v[88:91], 0\n\t"
      "ds_read_b64_tr_b16 v[114:115], v15 offset:23136\n\t"
      "v_xor_b32 v7, 160, v2\n\t"
      "v_mfma_f32_16x16x32_bf16 v[120:123], v[140:143], v[92:95], v[120:123]\n\t"
      "ds_read_b64_tr_b16 v[116:117], v15 offset:27744\n\t"
      "v_xor_b32 v8, 192, v2\n\t"
      "v_mfma_f32_16x16x32_bf16 v[124:127], v[156:159], v[92:95], v[124:127]\n\t"
      "ds_read_b64_tr_b16 v[118:119], v15 offset:32352\n\t"
      "v_mfma_f32_16x16x32_bf16 v[56:59], v[188:191], v[204:207], 0\n\t"
      "v_xor_b32 v9, 224, v2\n\t"
      "s_add_u32 s48, s48, 1\n\t"
      "v_mfma_f32_16x16x32_bf16 v[60:63], v[188:191], v[208:211], 0\n\t"
      "s_and_b32 s48, s48, 3\n\t"
      "s_mul_i32 s53, s48, 36864\n\t"
      "s_waitcnt lgkmcnt(8)\n\t"
      "v_mfma_f32_16x16x32_bf16 v[120:123], v[144:147], v[96:99], v[120:123]\n\t"
      "ds_read_b64_tr_b16 v[88:89], v15 offset:128\n\t"
      "v_cvt_pk_bf16_f32 v172, v128, v129\n\t"
      "v_mfma_f32_16x16x32_bf16 v[124:127], v[160:163], v[96:99], v[124:127]\n\t"
      "ds_read_b64_tr_b16 v[90:91], v15 offset:4736\n\t"
      "v_cvt_pk_bf16_f32 v173, v130, v131\n\t"
      "v_mfma_f32_16x16x32_bf16 v[120:123], v[148:151], v[100:103], v[120:123]\n\t"
      "ds_read_b64_tr_b16 v[92:93], v15 offset:9344\n\t"
      "v_cvt_pk_bf16_f32 v174, v132, v133\n\t"
      "v_mfma_f32_16x16x32_bf16 v[124:127], v[164:167], v[100:103], v[124:127]\n\t"
      "ds_read_b64_tr_b16 v[94:95], v15 offset:13952\n\t"
      "v_cvt_pk_bf16_f32 v175, v134, v135\n\t"
      "s_waitcnt lgkmcnt(8)\n\t"
      "v_mfma_f32_16x16x32_bf16 v[128:131], v[136:139], v[104:107], 0\n\t"
      "ds_read_b64_tr_b16 v[96:97], v15 offset:18560\n\t"
      "buffer_load_dwordx4 v[212:215], v13, s[44:47], s61 offen sc1\n\t"
      "v_mfma_f32_16x16x32_bf16 v[132:135], v[152:155], v[104:107], 0\n\t"
      "ds_read_b64_tr_b16 v[98:99], v15 offset:23168\n\t"
      "buffer_load_dwordx4 v[216:219], v13, s[44:47], s61 offen offset:1024 sc1\n\t"
      "v_mfma_f32_16x16x32_bf16 v[128:131], v[140:143], v[108:111], v[128:131]\n\t"
      "ds_read_b64_tr_b16 v[100:101], v15 offset:27776\n\t"
      "buffer_load_dwordx2 v[220:221], v14, s[44:47], s61 offen offset:2048 sc1\n\t"
      "v_mfma_f32_16x16x32_bf16 v[132:135], v[156:159], v[108:111], v[132:135]\n\t"
      "ds_read_b64_tr_b16 v[102:103], v15 offset:32384\n\t"
      "v_mfma_f32_16x16x32_bf16 v[64:67], v[192:195], v[204:207], 0\n\t"
      "buffer_load_dwordx4 v[222:225], v13, s[44:47], s62 offen sc1\n\t"
      "buffer_load_dwordx4 v[226:229], v13, s[44:47], s62 offen offset:1024 sc1\n\t"
      "v_mfma_f32_16x16x32_bf16 v[68:71], v[192:195], v[208:211], 0\n\t"
      "s_waitcnt lgkmcnt(8)\n\t"
      "v_mfma_f32_16x16x32_bf16 v[128:131], v[144:147], v[112:115], v[128:131]\n\t"
      "ds_read_b64_tr_b16 v[104:105], v15 offset:160\n\t"
      "v_cvt_pk_bf16_f32 v176, v120, v121\n\t"
      "v_mfma_f32_16x16x32_bf16 v[132:135], v[160:163], v[112:115], v[132:135]\n\t"
      "ds_read_b64_tr_b16 v[106:107], v15 offset:4768\n\t"
      "v_cvt_pk_bf16_f32 v177, v122, v123\n\t"
      "v_mfma_f32_16x16x32_bf16 v[128:131], v[148:151], v[116:119], v[128:131]\n\t"
      "ds_read_b64_tr_b16 v[108:109], v15 offset:9376\n\t"
      "v_cvt_pk_bf16_f32 v178, v124, v125\n\t"
      "v_mfma_f32_16x16x32_bf16 v[132:135], v[164:167], v[116:119], v[132:135]\n\t"
      "ds_read_b64_tr_b16 v[110:111], v15 offset:13984\n\t"
      "v_cvt_pk_bf16_f32 v179, v126, v127\n\t"
      "s_waitcnt lgkmcnt(8)\n\t"
      "v_mfma_f32_16x16x32_bf16 v[120:123], v[136:139], v[88:91], 0\n\t"
      "ds_read_b64_tr_b16 v[112:113], v15 offset:18592\n\t"
      "buffer_load_dwordx2 v[230:231], v14, s[44:47], s62 offen offset:2048 sc1\n\t"
      "v_mfma_f32_16x16x32_bf16 v[124:127], v[152:155], v[88:91], 0\n\t"
      "ds_read_b64_tr_b16 v[114:115], v15 offset:23200\n\t"
      "buffer_load_dwordx4 v[232:235], v13, s[44:47], s63 offen sc1\n\t"
      "v_mfma_f32_16x16x32_bf16 v[120:123], v[140:143], v[92:95], v[120:123]\n\t"
      "ds_read_b64_tr_b16 v[116:117], v15 offset:27808\n\t"
      "buffer_load_dwordx4 v[236:239], v13, s[44:47], s63 offen offset:1024 sc1\n\t"
      "v_mfma_f32_16x16x32_bf16 v[124:127], v[156:159], v[92:95], v[124:127]\n\t"
      "ds_read_b64_tr_b16 v[118:119], v15 offset:32416\n\t"
      "v_mfma_f32_16x16x32_bf16 v[72:75], v[196:199], v[204:207], 0\n\t"
      "buffer_load_dwordx2 v[240:241], v14, s[44:47], s63 offen offset:2048 sc1\n\t"
      "v_mfma_f32_16x16x32_bf16 v[76:79], v[196:199], v[208:211], 0\n\t"
      "s_waitcnt lgkmcnt(8)\n\t"
      "v_mfma_f32_16x16x32_bf16 v[120:123], v[144:147], v[96:99], v[120:123]\n\t"
      "ds_read_b64_tr_b16 v[88:89], v15 offset:192\n\t"
      "v_cvt_pk_bf16_f32 v180, v128, v129\n\t"
      "v_mfma_f32_16x16x32_bf16 v[124:127], v[160:163], v[96:99], v[124:127]\n\t"
      "ds_read_b64_tr_b16 v[90:91], v15 offset:4800\n\t"
      "v_cvt_pk_bf16_f32 v181, v130, v131\n\t"
      "v_mfma_f32_16x16x32_bf16 v[120:123], v[148:151], v[100:103], v[120:123]\n\t"
      "ds_read_b64_tr_b16 v[92:93], v15 offset:9408\n\t"
      "v_cvt_pk_bf16_f32 v182, v132, v133\n\t"
      "v_mfma_f32_16x16x32_bf16 v[124:127], v[164:167], v[100:103], v[124:127]\n\t"
      "ds_read_b64_tr_b16 v[94:95], v15 offset:14016\n\t"
      "v_cvt_pk_bf16_f32 v183, v134, v135\n\t"
      "s_waitcnt lgkmcnt(8)\n\t"
      "v_mfma_f32_16x16x32_bf16 v[128:131], v[136:139], v[104:107], 0\n\t"
      "ds_read_b64_tr_b16 v[96:97], v15 offset:18624\n\t"
      "v_mfma_f32_16x16x32_bf16 v[132:135], v[152:155], v[104:107], 0\n\t"
      "ds_read_b64_tr_b16 v[98:99], v15 offset:23232\n\t"
      "v_mfma_f32_16x16x32_bf16 v[128:131], v[140:143], v[108:111], v[128:131]\n\t"
      "ds_read_b64_tr_b16 v[100:101], v15 offset:27840\n\t"
      "v_mfma_f32_16x16x32_bf16 v[132:135], v[156:159], v[108:111], v[132:135]\n\t"
      "ds_read_b64_tr_b16 v[102:103], v15 offset:32448\n\t"
      "v_mfma_f32_16x16x32_bf16 v[80:83], v[200:203], v[204:207], 0\n\t"
      "v_mfma_f32_16x16x32_bf16 v[84:87], v[200:203], v[208:211], 0\n\t"
      "s_waitcnt lgkmcnt(8)\n\t"
      "v_mfma_f32_16x16x32_bf16 v[128:131], v[144:147], v[112:115], v[128:131]\n\t"
      "ds_read_b64_tr_b16 v[104:105], v15 offset:224\n\t"
      "v_cvt_pk_bf16_f32 v184, v120, v121\n\t"
      "v_mfma_f32_16x16x32_bf16 v[132:135], v[160:163], v[112:115], v[132:135]\n\t"
      "ds_read_b64_tr_b16 v[106:107], v15 offset:4832\n\t"
      "v_cvt_pk_bf16_f32 v185, v122, v123\n\t"
      "v_mfma_f32_16x16x32_bf16 v[128:131], v[148:151], v[116:119], v[128:131]\n\t"
      "ds_read_b64_tr_b16 v[108:109], v15 offset:9440\n\t"
      "v_cvt_pk_bf16_f32 v186, v124, v125\n\t"
      "v_mfma_f32_16x16x32_bf16 v[132:135], v[164:167], v[116:119], v[132:135]\n\t"
      "ds_read_b64_tr_b16 v[110:111], v15 offset:14048\n\t"
      "v_cvt_pk_bf16_f32 v187, v126, v127\n\t"
      "s_waitcnt lgkmcnt(8)\n\t"
      "v_mfma_f32_16x16x32_bf16 v[120:123], v[136:139], v[88:91], 0\n\t"
      "ds_read_b64_tr_b16 v[112:113], v15 offset:18656\n\t"
      "v_mfma_f32_16x16x32_bf16 v[124:127], v[152:155], v[88:91], 0\n\t"
      "ds_read_b64_tr_b16 v[114:115], v15 offset:23264\n\t"
      "v_mfma_f32_16x16x32_bf16 v[120:123], v[140:143], v[92:95], v[120:123]\n\t"
      "ds_read_b64_tr_b16 v[116:117], v15 offset:27872\n\t"
      "v_mfma_f32_16x16x32_bf16 v[124:127], v[156:159], v[92:95], v[124:127]\n\t"
      "ds_read_b64_tr_b16 v[118:119], v15 offset:32480\n\t"
      "s_waitcnt lgkmcnt(8)\n\t"
      "v_mfma_f32_16x16x32_bf16 v[120:123], v[144:147], v[96:99], v[120:123]\n\t"
      "ds_read_b64_tr_b16 v[88:89], v15 offset:256\n\t"
      "v_cvt_pk_bf16_f32 v188, v128, v129\n\t"
      "v_mfma_f32_16x16x32_bf16 v[124:127], v[160:163], v[96:99], v[124:127]\n\t"
      "ds_read_b64_tr_b16 v[90:91], v15 offset:4864\n\t"
      "v_cvt_pk_bf16_f32 v189, v130, v131\n\t"
      "v_mfma_f32_16x16x32_bf16 v[120:123], v[148:151], v[100:103], v[120:123]\n\t"
      "ds_read_b64_tr_b16 v[92:93], v15 offset:9472\n\t"
      "v_cvt_pk_bf16_f32 v190, v132, v133\n\t"
      "v_mfma_f32_16x16x32_bf16 v[124:127], v[164:167], v[100:103], v[124:127]\n\t"
      "ds_read_b64_tr_b16 v[94:95], v15 offset:14080\n\t"
      "v_cvt_pk_bf16_f32 v191, v134, v135\n\t"
      "s_waitcnt lgkmcnt(8)\n\t"
      "v_mfma_f32_16x16x32_bf16 v[128:131], v[136:139], v[104:107], 0\n\t"
      "ds_read_b64_tr_b16 v[96:97], v15 offset:18688\n\t"
      "v_mfma_f32_16x16x32_bf16 v[132:135], v[152:155], v[104:107], 0\n\t"
      "ds_read_b64_tr_b16 v[98:99], v15 offset:23296\n\t"
      "v_mfma_f32_16x16x32_bf16 v[128:131], v[140:143], v[108:111], v[128:131]\n\t"
      "ds_read_b64_tr_b16 v[100:101], v15 offset:27904\n\t"
      "v_mfma_f32_16x16x32_bf16 v[132:135], v[156:159], v[108:111], v[132:135]\n\t"
      "ds_read_b64_tr_b16 v[102:103], v15 offset:32512\n\t"
      "s_waitcnt lgkmcnt(8)\n\t"
      "v_mfma_f32_16x16x32_bf16 v[128:131], v[144:147], v[112:115], v[128:131]\n\t"
      "v_cvt_pk_bf16_f32 v192, v120, v121\n\t"
      "v_mfma_f32_16x16x32_bf16 v[132:135], v[160:163], v[112:115], v[132:135]\n\t"
      "v_cvt_pk_bf16_f32 v193, v122, v123\n\t"
      "v_mfma_f32_16x16x32_bf16 v[128:131], v[148:151], v[116:119], v[128:131]\n\t"
      "v_cvt_pk_bf16_f32 v194, v124, v125\n\t"
      "v_mfma_f32_16x16x32_bf16 v[132:135], v[164:167], v[116:119], v[132:135]\n\t"
      "v_cvt_pk_bf16_f32 v195, v126, v127\n\t"
      "s_waitcnt lgkmcnt(4)\n\t"
      "v_mfma_f32_16x16x32_bf16 v[120:123], v[136:139], v[88:91], 0\n\t"
      "s_waitcnt vmcnt(6)\n\t"
      "v_add3_u32 v250, v212, v214, v216\n\t"
      "v_mfma_f32_16x16x32_bf16 v[124:127], v[152:155], v[88:91], 0\n\t"
      "v_add3_u32 v251, v213, v215, v217\n\t"
      "v_add3_u32 v250, v250, v218, s40\n\t"
      "v_mfma_f32_16x16x32_bf16 v[120:123], v[140:143], v[92:95], v[120:123]\n\t"
      "v_add3_u32 v251, v251, v219, s41\n\t"
      "v_xor_b32 v250, v220, v250\n\t"
      "v_mfma_f32_16x16x32_bf16 v[124:127], v[156:159], v[92:95], v[124:127]\n\t"
      "v_bitop3_b32 v250, v251, v221, v250 bitop3:0xbe\n\t"
      "v_cmp_ne_u32 vcc, 0, v250\n\t"
      "s_cmp_eq_u64 vcc, 0\n\t"
      "s_cbranch_scc0 LPR1%=\n\tLPO1%=:\n\t"
      "s_waitcnt lgkmcnt(0)\n\t"
      "v_add_u32 v15, s53, v12\n\t"
      "s_barrier\n\t"
      "ds_read_b64 v[136:137], v2\n\t"
      "ds_read_b64 v[152:153], v2 offset:4096\n\t"
      "ds_read_b64 v[138:139], v3\n\t"
      "v_mfma_f32_16x16x32_bf16 v[120:123], v[144:147], v[96:99], v[120:123]\n\t"
      "v_cvt_pk_bf16_f32 v196, v128, v129\n\t"
      "ds_read_b64 v[154:155], v3 offset:4096\n\t"
      "ds_read_b64 v[140:141], v4\n\t"
      "ds_read_b64 v[156:157], v4 offset:4096\n\t"
      "v_mfma_f32_16x16x32_bf16 v[124:127], v[160:163], v[96:99], v[124:127]\n\t"
      "v_cvt_pk_bf16_f32 v197, v130, v131\n\t"
      "ds_read_b64 v[142:143], v5\n\t"
      "ds_read_b64 v[158:159], v5 offset:4096\n\t"
      "ds_read_b64_tr_b16 v[88:89], v15 offset:0\n\t"
      "v_mfma_f32_16x16x32_bf16 v[120:123], v[148:151], v[100:103], v[120:123]\n\t"
      "v_cvt_pk_bf16_f32 v198, v132, v133\n\t"
      "ds_read_b64_tr_b16 v[90:91], v15 offset:4608\n\t"
      "ds_read_b64_tr_b16 v[92:93], v15 offset:9216\n\t"
      "ds_read_b64_tr_b16 v[94:95], v15 offset:13824\n\t"
      "v_mfma_f32_16x16x32_bf16 v[124:127], v[164:167], v[100:103], v[124:127]\n\t"
      "v_cvt_pk_bf16_f32 v199, v134, v135\n\t"
      "s_add_u32 s48, s48, 1\n\t"
      "s_and_b32 s48, s48, 3\n\t"
      "v_mfma_f32_16x16x32_bf16 v[16:19], v[168:171], v[212:215], v[16:19]\n\t"
      "ds_read_b64 v[144:145], v6\n\t"
      "ds_read_b64 v[160:161], v6 offset:4096\n\t"
      "ds_read_b64 v[146:147], v7\n\t"
      "v_mfma_f32_16x16x32_bf16 v[20:23], v[168:171], v[216:219], v[20:23]\n\t"
      "ds_read_b64 v[162:163], v7 offset:4096\n\t"
      "ds_read_b64 v[148:149], v8\n\t"
      "ds_read_b64 v[164:165], v8 offset:4096\n\t"
      "v_mfma_f32_16x16x32_bf16 v[24:27], v[172:175], v[212:215], v[24:27]\n\t"
      "ds_read_b64 v[150:151], v9\n\t"
      "ds_read_b64 v[166:167], v9 offset:4096\n\t"
      "ds_read_b64_tr_b16 v[96:97], v15 offset:18432\n\t"
      "v_mfma_f32_16x16x32_bf16 v[28:31], v[172:175], v[216:219], v[28:31]\n\t"
      "ds_read_b64_tr_b16 v[98:99], v15 offset:23040\n\t"
      "ds_read_b64_tr_b16 v[100:101], v15 offset:27648\n\t"
      "ds_read_b64_tr_b16 v[102:103], v15 offset:32256\n\t"
      "v_mfma_f32_16x16x32_bf16 v[32:35], v[176:179], v[212:215], v[32:35]\n\t"
      "ds_read_b64_tr_b16 v[104:105], v15 offset:32\n\t"
      "ds_read_b64_tr_b16 v[106:107], v15 offset:4640\n\t"
      "v_cvt_pk_bf16_f32 v200, v120, v121\n\t"
      "v_cvt_pk_bf16_f32 v201, v122, v123\n\t"
      "v_mfma_f32_16x16x32_bf16 v[36:39], v[176:179], v[216:219], v[36:39]\n\t"
      "ds_read_b64_tr_b16 v[108:109], v15 offset:9248\n\t"
      "ds_read_b64_tr_b16 v[110:111], v15 offset:13856\n\t"
      "v_cvt_pk_bf16_f32 v202, v124, v125\n\t"
      "v_cvt_pk_bf16_f32 v203, v126, v127\n\t"
      "s_waitcnt lgkmcnt(15)\n\t"
      "v_mfma_f32_16x16x32_bf16 v[120:123], v[136:139], v[88:91], 0\n\t"
      "ds_read_b64_tr_b16 v[112:113], v15 offset:18464\n\t"
      "v_mfma_f32_16x16x32_bf16 v[124:127], v[152:155], v[88:91], 0\n\t"
      "ds_read_b64_tr_b16 v[114:115], v15 offset:23072\n\t"
      "v_mfma_f32_16x16x32_bf16 v[120:123], v[140:143], v[92:95], v[120:123]\n\t"
      "ds_read_b64_tr_b16 v[116:117], v15 offset:27680\n\t"
      "v_mfma_f32_16x16x32_bf16 v[124:127], v[156:159], v[92:95], v[124:127]\n\t"
      "ds_read_b64_tr_b16 v[118:119], v15 offset:32288\n\t"
      "v_mfma_f32_16x16x32_bf16 v[40:43], v[180:183], v[212:215], v[40:43]\n\t"
      "v_mfma_f32_16x16x32_bf16 v[44:47], v[180:183], v[216:219], v[44:47]\n\t"
      "s_waitcnt lgkmcnt(8)\n\t"
      "v_mfma_f32_16x16x32_bf16 v[120:123], v[144:147], v[96:99], v[120:123]\n\t"
      "ds_read_b64_tr_b16 v[88:89], v15 offset:64\n\t"
      "v_mfma_f32_16x16x32_bf16 v[124:127], v[160:163], v[96:99], v[124:127]\n\t"
      "ds_read_b64_tr_b16 v[90:91], v15 offset:4672\n\t"
      "v_mfma_f32_16x16x32_bf16 v[120:123], v[148:151], v[100:103], v[120:123]\n\t"
      "ds_read_b64_tr_b16 v[92:93], v15 offset:9280\n\t"
      "v_mfma_f32_16x16x32_bf16 v[124:127], v[164:167], v[100:103], v[124:127]\n\t"
      "ds_read_b64_tr_b16 v[94:95], v15 offset:13888\n\t"
      "s_waitcnt lgkmcnt(8)\n\t"
      "v_mfma_f32_16x16x32_bf16 v[128:131], v[136:139], v[104:107], 0\n\t"
      "ds_read_b64_tr_b16 v[96:97], v15 offset:18496\n\t"
      "s_mul_i32 s53, s48, 36864\n\t"
      "v_mfma_f32_16x16x32_bf16 v[132:135], v[152:155], v[104:107], 0\n\t"
      "ds_read_b64_tr_b16 v[98:99], v15 offset:23104\n\t"
      "v_add_u32 v2, s53, v11\n\t"
      "v_mfma_f32_16x16x32_bf16 v[128:131], v[140:143], v[108:111], v[128:131]\n\t"
      "ds_read_b64_tr_b16 v[100:101], v15 offset:27712\n\t"
      "v_xor_b32 v3, 32, v2\n\t"
      "v_mfma_f32_16x16x32_bf16 v[132:135], v[156:159], v[108:111], v[132:135]\n\t"
      "ds_read_b64_tr_b16 v[102:103], v15 offset:32320\n\t"
      "v_mfma_f32_16x16x32_bf16 v[48:51], v[184:187], v[212:215], v[48:51]\n\t"
      "v_xor_b32 v4, 64, v2\n\t"
      "v_xor_b32 v5, 96, v2\n\t"
      "v_mfma_f32_16x16x32_bf16 v[52:55], v[184:187], v[216:219], v[52:55]\n\t"
      "s_waitcnt lgkmcnt(8)\n\t"
      "v_mfma_f32_16x16x32_bf16 v[128:131], v[144:147], v[112:115], v[128:131]\n\t"
      "ds_read_b64_tr_b16 v[104:105], v15 offset:96\n\t"
      "v_cvt_pk_bf16_f32 v168, v120, v121\n\t"
      "v_mfma_f32_16x16x32_bf16 v[132:135], v[160:163], v[112:115], v[132:135]\n\t"
      "ds_read_b64_tr_b16 v[106:107], v15 offset:4704\n\t"
      "v_cvt_pk_bf16_f32 v169, v122, v123\n\t"
      "v_mfma_f32_16x16x32_bf16 v[128:131], v[148:151], v[116:119], v[128:131]\n\t"
      "ds_read_b64_tr_b16 v[108:109], v15 offset:9312\n\t"
      "v_cvt_pk_bf16_f32 v170, v124, v125\n\t"
      "v_mfma_f32_16x16x32_bf16 v[132:135], v[164:167], v[116:119], v[132:135]\n\t"
      "ds_read_b64_tr_b16 v[110:111], v15 offset:13920\n\t"
      "v_cvt_pk_bf16_f32 v171, v126, v127\n\t"
      "s_waitcnt lgkmcnt(8)\n\t"
      "v_mfma_f32_16x16x32_bf16 v[120:123], v[136:139], v[88:91], 0\n\t"
      "ds_read_b64_tr_b16 v[112:113], v15 offset:18528\n\t"
      "v_xor_b32 v6, 128, v2\n\t"
      "v_mfma_f32_16x16x32_bf16 v[124:127], v[152:155], v[88:91], 0\n\t"
      "ds_read_b64_tr_b16 v[114:115], v15 offset:23136\n\t"
      "v_xor_b32 v7, 160, v2\n\t"
      "v_mfma_f32_16x16x32_bf16 v[120:123], v[140:143], v[92:95], v[120:123]\n\t"
      "ds_read_b64_tr_b16 v[116:117], v15 offset:27744\n\t"
      "v_xor_b32 v8, 192, v2\n\t"
      "v_mfma_f32_16x16x32_bf16 v[124:127], v[156:159], v[92:95], v[124:127]\n\t"
      "ds_read_b64_tr_b16 v[118:119], v15 offset:32352\n\t"
      "v_mfma_f32_16x16x32_bf16 v[56:59], v[188:191], v[212:215], v[56:59]\n\t"
      "v_xor_b32 v9, 224, v2\n\t"
      "s_add_u32 s48, s48, 1\n\t"
      "v_mfma_f32_16x16x32_bf16 v[60:63], v[188:191], v[216:219], v[60:63]\n\t"
      "s_and_b32 s48, s48, 3\n\t"
      "s_mul_i32 s53, s48, 36864\n\t"
      "s_waitcnt lgkmcnt(8)\n\t"
      "v_mfma_f32_16x16x32_bf16 v[120:123], v[144:147], v[96:99], v[120:123]\n\t"
      "ds_read_b64_tr_b16 v[88:89], v15 offset:128\n\t"
      "v_cvt_pk_bf16_f32 v172, v128, v129\n\t"
      "v_mfma_f32_16x16x32_bf16 v[124:127], v[160:163], v[96:99], v[124:127]\n\t"
      "ds_read_b64_tr_b16 v[90:91], v15 offset:4736\n\t"
      "v_cvt_pk_bf16_f32 v173, v130, v131\n\t"
      "v_mfma_f32_16x16x32_bf16 v[120:123], v[148:151], v[100:103], v[120:123]\n\t"
      "ds_read_b64_tr_b16 v[92:93], v15 offset:9344\n\t"
      "v_cvt_pk_bf16_f32 v174, v132, v133\n\t"
      "v_mfma_f32_16x16x32_bf16 v[124:127], v[164:167], v[100:103], v[124:127]\n\t"
      "ds_read_b64_tr_b16 v[94:95], v15 offset:13952\n\t"
      "v_cvt_pk_bf16_f32 v175, v134, v135\n\t"
      "s_waitcnt lgkmcnt(8)\n\t"
      "v_mfma_f32_16x16x32_bf16 v[128:131], v[136:139], v[104:107], 0\n\t"
      "ds_read_b64_tr_b16 v[96:97], v15 offset:18560\n\t"
      "v_mfma_f32_16x16x32_bf16 v[132:135], v[152:155], v[104:107], 0\n\t"
      "ds_read_b64_tr_b16 v[98:99], v15 offset:23168\n\t"
      "v_mfma_f32_16x16x32_bf16 v[128:131], v[140:143], v[108:111], v[128:131]\n\t"
      "ds_read_b64_tr_b16 v[100:101], v15 offset:27776\n\t"
      "v_mfma_f32_16x16x32_bf16 v[132:135], v[156:159], v[108:111], v[132:135]\n\t"
      "ds_read_b64_tr_b16 v[102:103], v15 offset:32384\n\t"
      "v_mfma_f32_16x16x32_bf16 v[64:67], v[192:195], v[212:215], v[64:67]\n\t"
      "v_mfma_f32_16x16x32_bf16 v[68:71], v[192:195], v[216:219], v[68:71]\n\t"
      "s_waitcnt lgkmcnt(8)\n\t"
      "v_mfma_f32_16x16x32_bf16 v[128:131], v[144:147], v[112:115], v[128:131]\n\t"
      "ds_read_b64_tr_b16 v[104:105], v15 offset:160\n\t"
      "v_cvt_pk_bf16_f32 v176, v120, v121\n\t"
      "v_mfma_f32_16x16x32_bf16 v[132:135], v[160:163], v[112:115], v[132:135]\n\t"
      "ds_read_b64_tr_b16 v[106:107], v15 offset:4768\n\t"
      "v_cvt_pk_bf16_f32 v177, v122, v123\n\t"
      "v_mfma_f32_16x16x32_bf16 v[128:131], v[148:151], v[116:119], v[128:131]\n\t"
      "ds_read_b64_tr_b16 v[108:109], v15 offset:9376\n\t"
      "v_cvt_pk_bf16_f32 v178, v124, v125\n\t"
      "v_mfma_f32_16x16x32_bf16 v[132:135], v[164:167], v[116:119], v[132:135]\n\t"
      "ds_read_b64_tr_b16 v[110:111], v15 offset:13984\n\t"
      "v_cvt_pk_bf16_f32 v179, v126, v127\n\t"
      "s_waitcnt lgkmcnt(8)\n\t"
      "v_mfma_f32_16x16x32_bf16 v[120:123], v[136:139], v[88:91], 0\n\t"
      "ds_read_b64_tr_b16 v[112:113], v15 offset:18592\n\t"
      "v_mfma_f32_16x16x32_bf16 v[124:127], v[152:155], v[88:91], 0\n\t"
      "ds_read_b64_tr_b16 v[114:115], v15 offset:23200\n\t"
      "v_mfma_f32_16x16x32_bf16 v[120:123], v[140:143], v[92:95], v[120:123]\n\t"
      "ds_read_b64_tr_b16 v[116:117], v15 offset:27808\n\t"
      "v_mfma_f32_16x16x32_bf16 v[124:127], v[156:159], v[92:95], v[124:127]\n\t"
      "ds_read_b64_tr_b16 v[118:119], v15 offset:32416\n\t"
      "v_mfma_f32_16x16x32_bf16 v[72:75], v[196:199], v[212:215], v[72:75]\n\t"
      "v_mfma_f32_16x16x32_bf16 v[76:79], v[196:199], v[216:219], v[76:79]\n\t"
      "s_waitcnt lgkmcnt(8)\n\t"
      "v_mfma_f32_16x16x32_bf16 v[120:123], v[144:147], v[96:99], v[120:123]\n\t"
      "ds_read_b64_tr_b16 v[88:89], v15 offset:192\n\t"
      "v_cvt_pk_bf16_f32 v180, v128, v129\n\t"
      "v_mfma_f32_16x16x32_bf16 v[124:127], v[160:163], v[96:99], v[124:127]\n\t"
      "ds_read_b64_tr_b16 v[90:91], v15 offset:4800\n\t"
      "v_cvt_pk_bf16_f32 v181, v130, v131\n\t"
      "v_mfma_f32_16x16x32_bf16 v[120:123], v[148:151], v[100:103], v[120:123]\n\t"
      "ds_read_b64_tr_b16 v[92:93], v15 offset:9408\n\t"
      "v_cvt_pk_bf16_f32 v182, v132, v133\n\t"
      "v_mfma_f32_16x16x32_bf16 v[124:127], v[164:167], v[100:103], v[124:127]\n\t"
      "ds_read_b64_tr_b16 v[94:95], v15 offset:14016\n\t"
      "v_cvt_pk_bf16_f32 v183, v134, v135\n\t"
      "s_waitcnt lgkmcnt(8)\n\t"
      "v_mfma_f32_16x16x32_bf16 v[128:131], v[136:139], v[104:107], 0\n\t"
      "ds_read_b64_tr_b16 v[96:97], v15 offset:18624\n\t"
      "v_mfma_f32_16x16x32_bf16 v[132:135], v[152:155], v[104:107], 0\n\t"
      "ds_read_b64_tr_b16 v[98:99], v15 offset:23232\n\t"
      "v_mfma_f32_16x16x32_bf16 v[128:131], v[140:143], v[108:111], v[128:131]\n\t"
      "ds_read_b64_tr_b16 v[100:101], v15 offset:27840\n\t"
      "v_mfma_f32_16x16x32_bf16 v[132:135], v[156:159], v[108:111], v[132:135]\n\t"
      "ds_read_b64_tr_b16 v[102:103], v15 offset:32448\n\t"
      "v_mfma_f32_16x16x32_bf16 v[80:83], v[200:203], v[212:215], v[80:83]\n\t"
      "v_mfma_f32_16x16x32_bf16 v[84:87], v[200:203], v[216:219], v[84:87]\n\t"
      "s_waitcnt lgkmcnt(8)\n\t"
      "v_mfma_f32_16x16x32_bf16 v[128:131], v[144:147], v[112:115], v[128:131]\n\t"
      "ds_read_b64_tr_b16 v[104:105], v15 offset:224\n\t"
      "v_cvt_pk_bf16_f32 v184, v120, v121\n\t"
      "v_mfma_f32_16x16x32_bf16 v[132:135], v[160:163], v[112:115], v[132:135]\n\t"
      "ds_read_b64_tr_b16 v[106:107], v15 offset:4832\n\t"
      "v_cvt_pk_bf16_f32 v185, v122, v123\n\t"
      "v_mfma_f32_16x16x32_bf16 v[128:131], v[148:151], v[116:119], v[128:131]\n\t"
      "ds_read_b64_tr_b16 v[108:109], v15 offset:9440\n\t"
      "v_cvt_pk_bf16_f32 v186, v124, v125\n\t"
      "v_mfma_f32_16x16x32_bf16 v[132:135], v[164:167], v[116:119], v[132:135]\n\t"
      "ds_read_b64_tr_b16 v[110:111], v15 offset:14048\n\t"
      "v_cvt_pk_bf16_f32 v187, v126, v127\n\t"
      "s_waitcnt lgkmcnt(8)\n\t"
      "v_mfma_f32_16x16x32_bf16 v[120:123], v[136:139], v[88:91], 0\n\t"
      "ds_read_b64_tr_b16 v[112:113], v15 offset:18656\n\t"
      "s_waitcnt vmcnt(0)\n\t"
      "v_mfma_f32_16x16x32_bf16 v[124:127], v[152:155], v[88:91], 0\n\t"
      "ds_read_b64_tr_b16 v[114:115], v15 offset:23264\n\t"
      "v_add3_u32 v250, v222, v224, v226\n\t"
      "v_mfma_f32_16x16x32_bf16 v[120:123], v[140:143], v[92:95], v[120:123]\n\t"
      "ds_read_b64_tr_b16 v[116:117], v15 offset:27872\n\t"
      "v_add3_u32 v251, v223, v225, v227\n\t"
      "v_mfma_f32_16x16x32_bf16 v[124:127], v[156:159], v[92:95], v[124:127]\n\t"
      "ds_read_b64_tr_b16 v[118:119], v15 offset:32480\n\t"
      "s_waitcnt lgkmcnt(8)\n\t"
      "v_mfma_f32_16x16x32_bf16 v[120:123], v[144:147], v[96:99], v[120:123]\n\t"
      "ds_read_b64_tr_b16 v[88:89], v15 offset:256\n\t"
      "v_cvt_pk_bf16_f32 v188, v128, v129\n\t"
      "v_mfma_f32_16x16x32_bf16 v[124:127], v[160:163], v[96:99], v[124:127]\n\t"
      "ds_read_b64_tr_b16 v[90:91], v15 offset:4864\n\t"
      "v_cvt_pk_bf16_f32 v189, v130, v131\n\t"
      "v_mfma_f32_16x16x32_bf16 v[120:123], v[148:151], v[100:103], v[120:123]\n\t"
      "ds_read_b64_tr_b16 v[92:93], v15 offset:9472\n\t"
      "v_cvt_pk_bf16_f32 v190, v132, v133\n\t"
      "v_mfma_f32_16x16x32_bf16 v[124:127], v[164:167], v[100:103], v[124:127]\n\t"
      "ds_read_b64_tr_b16 v[94:95], v15 offset:14080\n\t"
      "v_cvt_pk_bf16_f32 v191, v134, v135\n\t"
      "s_waitcnt lgkmcnt(8)\n\t"
      "v_mfma_f32_16x16x32_bf16 v[128:131], v[136:139], v[104:107], 0\n\t"
      "ds_read_b64_tr_b16 v[96:97], v15 offset:18688\n\t"
      "v_add3_u32 v250, v250, v228, s40\n\t"
      "v_mfma_f32_16x16x32_bf16 v[132:135], v[152:155], v[104:107], 0\n\t"
      "ds_read_b64_tr_b16 v[98:99], v15 offset:23296\n\t"
      "v_add3_u32 v251, v251, v229, s41\n\t"
      "v_mfma_f32_16x16x32_bf16 v[128:131], v[140:143], v[108:111], v[128:131]\n\t"
      "ds_read_b64_tr_b16 v[100:101], v15 offset:27904\n\t"
      "v_xor_b32 v250, v230, v250\n\t"
      "v_mfma_f32_16x16x32_bf16 v[132:135], v[156:159], v[108:111], v[132:135]\n\t"
      "ds_read_b64_tr_b16 v[102:103], v15 offset:32512\n\t"
      "s_waitcnt lgkmcnt(8)\n\t"
      "v_mfma_f32_16x16x32_bf16 v[128:131], v[144:147], v[112:115], v[128:131]\n\t"
      "v_bitop3_b32 v250, v251, v231, v250 bitop3:0xbe\n\t"
      "v_cvt_pk_bf16_f32 v192, v120, v121\n\t"
      "v_mfma_f32_16x16x32_bf16 v[132:135], v[160:163], v[112:115], v[132:135]\n\t"
      "v_cmp_ne_u32 vcc, 0, v250\n\t"
      "v_cvt_pk_bf16_f32 v193, v122, v123\n\t"
      "v_mfma_f32_16x16x32_bf16 v[128:131], v[148:151], v[116:119], v[128:131]\n\t"
      "s_cmp_eq_u64 vcc, 0\n\t"
      "v_cvt_pk_bf16_f32 v194, v124, v125\n\t"
      "v_mfma_f32_16x16x32_bf16 v[132:135], v[164:167], v[116:119], v[132:135]\n\t"
      "s_cbranch_scc0 LPR2%=\n\tLPO2%=:\n\t"
      "v_cvt_pk_bf16_f32 v195, v126, v127\n\t"
      "s_waitcnt lgkmcnt(4)\n\t"
      "v_mfma_f32_16x16x32_bf16 v[120:123], v[136:139], v[88:91], 0\n\t"
      "v_add3_u32 v250, v232, v234, v236\n\t"
      "v_add3_u32 v251, v233, v235, v237\n\t"
      "v_mfma_f32_16x16x32_bf16 v[124:127], v[152:155], v[88:91], 0\n\t"
      "v_add3_u32 v250, v250, v238, s40\n\t"
      "v_add3_u32 v251, v251, v239, s41\n\t"
      "v_mfma_f32_16x16x32_bf16 v[120:123], v[140:143], v[92:95], v[120:123]\n\t"
      "v_xor_b32 v250, v240, v250\n\t"
      "v_bitop3_b32 v250, v251, v241, v250 bitop3:0xbe\n\t"
      "v_mfma_f32_16x16x32_bf16 v[124:127], v[156:159], v[92:95], v[124:127]\n\t"
      "v_cmp_ne_u32 vcc, 0, v250\n\t"
      "s_cmp_eq_u64 vcc, 0\n\t"
      "s_cbranch_scc0 LPR3%=\n\tLPO3%=:\n\t"
      "s_waitcnt lgkmcnt(0)\n\t"
      "v_add_u32 v15, s53, v12\n\t"
      "s_barrier\n\t"
      "ds_read_b64 v[136:137], v2\n\t"
      "ds_read_b64 v[152:153], v2 offset:4096\n\t"
      "ds_read_b64 v[138:139], v3\n\t"
      "v_mfma_f32_16x16x32_bf16 v[120:123], v[144:147], v[96:99], v[120:123]\n\t"
      "v_cvt_pk_bf16_f32 v196, v128, v129\n\t"
      "ds_read_b64 v[154:155], v3 offset:4096\n\t"
      "ds_read_b64 v[140:141], v4\n\t"
      "ds_read_b64 v[156:157], v4 offset:4096\n\t"
      "v_mfma_f32_16x16x32_bf16 v[124:127], v[160:163], v[96:99], v[124:127]\n\t"
      "v_cvt_pk_bf16_f32 v197, v130, v131\n\t"
      "ds_read_b64 v[142:143], v5\n\t"
      "ds_read_b64 v[158:159], v5 offset:4096\n\t"
      "ds_read_b64_tr_b16 v[88:89], v15 offset:0\n\t"
      "v_mfma_f32_16x16x32_bf16 v[120:123], v[148:151], v[100:103], v[120:123]\n\t"
      "v_cvt_pk_bf16_f32 v198, v132, v133\n\t"
      "ds_read_b64_tr_b16 v[90:91], v15 offset:4608\n\t"
      "ds_read_b64_tr_b16 v[92:93], v15 offset:9216\n\t"
      "ds_read_b64_tr_b16 v[94:95], v15 offset:13824\n\t"
      "v_mfma_f32_16x16x32_bf16 v[124:127], v[164:167], v[100:103], v[124:127]\n\t"
      "v_cvt_pk_bf16_f32 v199, v134, v135\n\t"
      "s_add_u32 s48, s48, 1\n\t"
      "s_and_b32 s48, s48, 3\n\t"
      "v_mfma_f32_16x16x32_bf16 v[16:19], v[168:171], v[222:225], v[16:19]\n\t"
      "ds_read_b64 v[144:145], v6\n\t"
      "ds_read_b64 v[160:161], v6 offset:4096\n\t"
      "ds_read_b64 v[146:147], v7\n\t"
      "v_mfma_f32_16x16x32_bf16 v[20:23], v[168:171], v[226:229], v[20:23]\n\t"
      "ds_read_b64 v[162:163], v7 offset:4096\n\t"
      "ds_read_b64 v[148:149], v8\n\t"
      "ds_read_b64 v[164:165], v8 offset:4096\n\t"
      "v_mfma_f32_16x16x32_bf16 v[24:27], v[172:175], v[222:225], v[24:27]\n\t"
      "ds_read_b64 v[150:151], v9\n\t"
      "ds_read_b64 v[166:167], v9 offset:4096\n\t"
      "ds_read_b64_tr_b16 v[96:97], v15 offset:18432\n\t"
      "v_mfma_f32_16x16x32_bf16 v[28:31], v[172:175], v[226:229], v[28:31]\n\t"
      "ds_read_b64_tr_b16 v[98:99], v15 offset:23040\n\t"
      "ds_read_b64_tr_b16 v[100:101], v15 offset:27648\n\t"
      "ds_read_b64_tr_b16 v[102:103], v15 offset:32256\n\t"
      "v_mfma_f32_16x16x32_bf16 v[32:35], v[176:179], v[222:225], v[32:35]\n\t"
      "ds_read_b64_tr_b16 v[104:105], v15 offset:32\n\t"
      "ds_read_b64_tr_b16 v[106:107], v15 offset:4640\n\t"
      "v_cvt_pk_bf16_f32 v200, v120, v121\n\t"
      "v_cvt_pk_bf16_f32 v201, v122, v123\n\t"
      "v_mfma_f32_16x16x32_bf16 v[36:39], v[176:179], v[226:229], v[36:39]\n\t"
      "ds_read_b64_tr_b16 v[108:109], v15 offset:9248\n\t"
      "ds_read_b64_tr_b16 v[110:111], v15 offset:13856\n\t"
      "v_cvt_pk_bf16_f32 v202, v124, v125\n\t"
      "v_cvt_pk_bf16_f32 v203, v126, v127\n\t"
      "s_waitcnt lgkmcnt(15)\n\t"
      "v_mfma_f32_16x16x32_bf16 v[120:123], v[136:139], v[88:91], 0\n\t"
      "ds_read_b64_tr_b16 v[112:113], v15 offset:18464\n\t"
      "v_mfma_f32_16x16x32_bf16 v[124:127], v[152:155], v[88:91], 0\n\t"
      "ds_read_b64_tr_b16 v[114:115], v15 offset:23072\n\t"
      "v_mfma_f32_16x16x32_bf16 v[120:123], v[140:143], v[92:95], v[120:123]\n\t"
      "ds_read_b64_tr_b16 v[116:117], v15 offset:27680\n\t"
      "v_mfma_f32_16x16x32_bf16 v[124:127], v[156:159], v[92:95], v[124:127]\n\t"
      "ds_read_b64_tr_b16 v[118:119], v15 offset:32288\n\t"
      "v_mfma_f32_16x16x32_bf16 v[40:43], v[180:183], v[222:225], v[40:43]\n\t"
      "v_mfma_f32_16x16x32_bf16 v[44:47], v[180:183], v[226:229], v[44:47]\n\t"
      "s_waitcnt lgkmcnt(8)\n\t"
      "v_mfma_f32_16x16x32_bf16 v[120:123], v[144:147], v[96:99], v[120:123]\n\t"
      "ds_read_b64_tr_b16 v[88:89], v15 offset:64\n\t"
      "v_mfma_f32_16x16x32_bf16 v[124:127], v[160:163], v[96:99], v[124:127]\n\t"
      "ds_read_b64_tr_b16 v[90:91], v15 offset:4672\n\t"
      "v_mfma_f32_16x16x32_bf16 v[120:123], v[148:151], v[100:103], v[120:123]\n\t"
      "ds_read_b64_tr_b16 v[92:93], v15 offset:9280\n\t"
      "v_mfma_f32_16x16x32_bf16 v[124:127], v[164:167], v[100:103], v[124:127]\n\t"
      "ds_read_b64_tr_b16 v[94:95], v15 offset:13888\n\t"
      "s_waitcnt lgkmcnt(8)\n\t"
      "v_mfma_f32_16x16x32_bf16 v[128:131], v[136:139], v[104:107], 0\n\t"
      "ds_read_b64_tr_b16 v[96:97], v15 offset:18496\n\t"
      "v_mfma_f32_16x16x32_bf16 v[132:135], v[152:155], v[104:107], 0\n\t"
      "ds_read_b64_tr_b16 v[98:99], v15 offset:23104\n\t"
      "v_mfma_f32_16x16x32_bf16 v[128:131], v[140:143], v[108:111], v[128:131]\n\t"
      "ds_read_b64_tr_b16 v[100:101], v15 offset:27712\n\t"
      "v_mfma_f32_16x16x32_bf16 v[132:135], v[156:159], v[108:111], v[132:135]\n\t"
      "ds_read_b64_tr_b16 v[102:103], v15 offset:32320\n\t"
      "v_mfma_f32_16x16x32_bf16 v[48:51], v[184:187], v[222:225], v[48:51]\n\t"
      "v_mfma_f32_16x16x32_bf16 v[52:55], v[184:187], v[226:229], v[52:55]\n\t"
      "s_waitcnt lgkmcnt(8)\n\t"
      "v_mfma_f32_16x16x32_bf16 v[128:131], v[144:147], v[112:115], v[128:131]\n\t"
      "ds_read_b64_tr_b16 v[104:105], v15 offset:96\n\t"
      "v_cvt_pk_bf16_f32 v168, v120, v121\n\t"
      "v_mfma_f32_16x16x32_bf16 v[132:135], v[160:163], v[112:115], v[132:135]\n\t"
      "ds_read_b64_tr_b16 v[106:107], v15 offset:4704\n\t"
      "v_cvt_pk_bf16_f32 v169, v122, v123\n\t"
      "v_mfma_f32_16x16x32_bf16 v[128:131], v[148:151], v[116:119], v[128:131]\n\t"
      "ds_read_b64_tr_b16 v[108:109], v15 offset:9312\n\t"
      "v_cvt_pk_bf16_f32 v170, v124, v125\n\t"
      "v_mfma_f32_16x16x32_bf16 v[132:135], v[164:167], v[116:119], v[132:135]\n\t"
      "ds_read_b64_tr_b16 v[110:111], v15 offset:13920\n\t"
      "v_cvt_pk_bf16_f32 v171, v126, v127\n\t"
      "s_waitcnt lgkmcnt(8)\n\t"
      "v_mfma_f32_16x16x32_bf16 v[120:123], v[136:139], v[88:91], 0\n\t"
      "ds_read_b64_tr_b16 v[112:113], v15 offset:18528\n\t"
      "v_mfma_f32_16x16x32_bf16 v[124:127], v[152:155], v[88:91], 0\n\t"
      "ds_read_b64_tr_b16 v[114:115], v15 offset:23136\n\t"
      "v_mfma_f32_16x16x32_bf16 v[16:19], v[168:171], v[232:235], v[16:19]\n\t"
      "v_mfma_f32_16x16x32_bf16 v[20:23], v[168:171], v[236:239], v[20:23]\n\t"
      "v_mfma_f32_16x16x32_bf16 v[120:123], v[140:143], v[92:95], v[120:123]\n\t"
      "ds_read_b64_tr_b16 v[116:117], v15 offset:27744\n\t"
      "v_mfma_f32_16x16x32_bf16 v[124:127], v[156:159], v[92:95], v[124:127]\n\t"
      "ds_read_b64_tr_b16 v[118:119], v15 offset:32352\n\t"
      "v_mfma_f32_16x16x32_bf16 v[56:59], v[188:191], v[222:225], v[56:59]\n\t"
      "v_mfma_f32_16x16x32_bf16 v[60:63], v[188:191], v[226:229], v[60:63]\n\t"
      "s_waitcnt lgkmcnt(8)\n\t"
      "v_mfma_f32_16x16x32_bf16 v[120:123], v[144:147], v[96:99], v[120:123]\n\t"
      "ds_read_b64_tr_b16 v[88:89], v15 offset:128\n\t"
      "v_cvt_pk_bf16_f32 v172, v128, v129\n\t"
      "v_mfma_f32_16x16x32_bf16 v[124:127], v[160:163], v[96:99], v[124:127]\n\t"
      "ds_read_b64_tr_b16 v[90:91], v15 offset:4736\n\t"
      "v_cvt_pk_bf16_f32 v173, v130, v131\n\t"
      "v_mfma_f32_16x16x32_bf16 v[120:123], v[148:151], v[100:103], v[120:123]\n\t"
      "ds_read_b64_tr_b16 v[92:93], v15 offset:9344\n\t"
      "v_cvt_pk_bf16_f32 v174, v132, v133\n\t"
      "v_mfma_f32_16x16x32_bf16 v[124:127], v[164:167], v[100:103], v[124:127]\n\t"
      "ds_read_b64_tr_b16 v[94:95], v15 offset:13952\n\t"
      "v_cvt_pk_bf16_f32 v175, v134, v135\n\t"
      "s_waitcnt lgkmcnt(8)\n\t"
      "v_mfma_f32_16x16x32_bf16 v[128:131], v[136:139], v[104:107], 0\n\t"
      "ds_read_b64_tr_b16 v[96:97], v15 offset:18560\n\t"
      "v_mfma_f32_16x16x32_bf16 v[132:135], v[152:155], v[104:107], 0\n\t"
      "ds_read_b64_tr_b16 v[98:99], v15 offset:23168\n\t"
      "v_mfma_f32_16x16x32_bf16 v[24:27], v[172:175], v[232:235], v[24:27]\n\t"
      "v_mfma_f32_16x16x32_bf16 v[28:31], v[172:175], v[236:239], v[28:31]\n\t"
      "v_mfma_f32_16x16x32_bf16 v[128:131], v[140:143], v[108:111], v[128:131]\n\t"
      "ds_read_b64_tr_b16 v[100:101], v15 offset:27776\n\t"
      "v_mfma_f32_16x16x32_bf16 v[132:135], v[156:159], v[108:111], v[132:135]\n\t"
      "ds_read_b64_tr_b16 v[102:103], v15 offset:32384\n\t"
      "v_mfma_f32_16x16x32_bf16 v[64:67], v[192:195], v[222:225], v[64:67]\n\t"
      "v_mfma_f32_16x16x32_bf16 v[68:71], v[192:195], v[226:229], v[68:71]\n\t"
      "s_waitcnt lgkmcnt(8)\n\t"
      "v_mfma_f32_16x16x32_bf16 v[128:131], v[144:147], v[112:115], v[128:131]\n\t"
      "ds_read_b64_tr_b16 v[104:105], v15 offset:160\n\t"
      "v_cvt_pk_bf16_f32 v176, v120, v121\n\t"
      "v_mfma_f32_16x16x32_bf16 v[132:135], v[160:163], v[112:115], v[132:135]\n\t"
      "ds_read_b64_tr_b16 v[106:107], v15 offset:4768\n\t"
      "v_cvt_pk_bf16_f32 v177, v122, v123\n\t"
      "v_mfma_f32_16x16x32_bf16 v[128:131], v[148:151], v[116:119], v[128:131]\n\t"
      "ds_read_b64_tr_b16 v[108:109], v15 offset:9376\n\t"
      "v_cvt_pk_bf16_f32 v178, v124, v125\n\t"
      "v_mfma_f32_16x16x32_bf16 v[132:135], v[164:167], v[116:119], v[132:135]\n\t"
      "ds_read_b64_tr_b16 v[110:111], v15 offset:13984\n\t"
      "v_cvt_pk_bf16_f32 v179, v126, v127\n\t"
      "s_waitcnt lgkmcnt(8)\n\t"
      "v_mfma_f32_16x16x32_bf16 v[120:123], v[136:139], v[88:91], 0\n\t"
      "ds_read_b64_tr_b16 v[112:113], v15 offset:18592\n\t"
      "v_cvt_pk_bf16_f32 v2, v16, v17\n\t"
      "v_mfma_f32_16x16x32_bf16 v[124:127], v[152:155], v[88:91], 0\n\t"
      "ds_read_b64_tr_b16 v[114:115], v15 offset:23200\n\t"
      "v_cvt_pk_bf16_f32 v3, v18, v19\n\t"
      "v_mfma_f32_16x16x32_bf16 v[32:35], v[176:179], v[232:235], v[32:35]\n\t"
      "v_cvt_pk_bf16_f32 v4, v24, v25\n\t"
      "v_cvt_pk_bf16_f32 v5, v26, v27\n\t"
      "v_mfma_f32_16x16x32_bf16 v[36:39], v[176:179], v[236:239], v[36:39]\n\t"
      "buffer_store_dwordx4 v[2:5], v13, s[64:67], 0 offen offset:0 sc1\n\t"
      "v_add3_u32 v242, v2, v4, s40\n\t"
      "v_mfma_f32_16x16x32_bf16 v[120:123], v[140:143], v[92:95], v[120:123]\n\t"
      "ds_read_b64_tr_b16 v[116:117], v15 offset:27808\n\t"
      "v_add3_u32 v243, v3, v5, s41\n\t"
      "v_mfma_f32_16x16x32_bf16 v[124:127], v[156:159], v[92:95], v[124:127]\n\t"
      "ds_read_b64_tr_b16 v[118:119], v15 offset:32416\n\t"
      "v_mfma_f32_16x16x32_bf16 v[72:75], v[196:199], v[222:225], v[72:75]\n\t"
      "v_cvt_pk_bf16_f32 v6, v20, v21\n\t"
      "v_cvt_pk_bf16_f32 v7, v22, v23\n\t"
      "v_mfma_f32_16x16x32_bf16 v[76:79], v[196:199], v[226:229], v[76:79]\n\t"
      "v_cvt_pk_bf16_f32 v8, v28, v29\n\t"
      "v_cvt_pk_bf16_f32 v9, v30, v31\n\t"
      "s_waitcnt lgkmcnt(8)\n\t"
      "v_mfma_f32_16x16x32_bf16 v[120:123], v[144:147], v[96:99], v[120:123]\n\t"
      "ds_read_b64_tr_b16 v[88:89], v15 offset:192\n\t"
      "v_cvt_pk_bf16_f32 v180, v128, v129\n\t"
      "v_mfma_f32_16x16x32_bf16 v[124:127], v[160:163], v[96:99], v[124:127]\n\t"
      "ds_read_b64_tr_b16 v[90:91], v15 offset:4800\n\t"
      "v_cvt_pk_bf16_f32 v181, v130, v131\n\t"
      "v_mfma_f32_16x16x32_bf16 v[120:123], v[148:151], v[100:103], v[120:123]\n\t"
      "ds_read_b64_tr_b16 v[92:93], v15 offset:9408\n\t"
      "v_cvt_pk_bf16_f32 v182, v132, v133\n\t"
      "v_mfma_f32_16x16x32_bf16 v[124:127], v[164:167], v[100:103], v[124:127]\n\t"
      "ds_read_b64_tr_b16 v[94:95], v15 offset:14016\n\t"
      "v_cvt_pk_bf16_f32 v183, v134, v135\n\t"
      "s_waitcnt lgkmcnt(8)\n\t"
      "v_mfma_f32_16x16x32_bf16 v[128:131], v[136:139], v[104:107], 0\n\t"
      "ds_read_b64_tr_b16 v[96:97], v15 offset:18624\n\t"
      "buffer_store_dwordx4 v[6:9], v13, s[68:71], 0 offen offset:0 sc1\n\t"
      "v_mfma_f32_16x16x32_bf16 v[132:135], v[152:155], v[104:107], 0\n\t"
      "ds_read_b64_tr_b16 v[98:99], v15 offset:23232\n\t"
      "v_add3_u32 v244, v6, v8, s40\n\t"
      "v_mfma_f32_16x16x32_bf16 v[40:43], v[180:183], v[232:235], v[40:43]\n\t"
      "v_add3_u32 v245, v7, v9, s41\n\t"
      "v_mfma_f32_16x16x32_bf16 v[44:47], v[180:183], v[236:239], v[44:47]\n\t"
      "v_mfma_f32_16x16x32_bf16 v[128:131], v[140:143], v[108:111], v[128:131]\n\t"
      "ds_read_b64_tr_b16 v[100:101], v15 offset:27840\n\t"
      "v_mfma_f32_16x16x32_bf16 v[132:135], v[156:159], v[108:111], v[132:135]\n\t"
      "ds_read_b64_tr_b16 v[102:103], v15 offset:32448\n\t"
      "v_mfma_f32_16x16x32_bf16 v[80:83], v[200:203], v[222:225], v[80:83]\n\t"
      "v_mfma_f32_16x16x32_bf16 v[84:87], v[200:203], v[226:229], v[84:87]\n\t"
      "s_waitcnt lgkmcnt(8)\n\t"
      "v_mfma_f32_16x16x32_bf16 v[128:131], v[144:147], v[112:115], v[128:131]\n\t"
      "ds_read_b64_tr_b16 v[104:105], v15 offset:224\n\t"
      "v_cvt_pk_bf16_f32 v184, v120, v121\n\t"
      "v_mfma_f32_16x16x32_bf16 v[132:135], v[160:163], v[112:115], v[132:135]\n\t"
      "ds_read_b64_tr_b16 v[106:107], v15 offset:4832\n\t"
      "v_cvt_pk_bf16_f32 v185, v122, v123\n\t"
      "v_mfma_f32_16x16x32_bf16 v[128:131], v[148:151], v[116:119], v[128:131]\n\t"
      "ds_read_b64_tr_b16 v[108:109], v15 offset:9440\n\t"
      "v_cvt_pk_bf16_f32 v186, v124, v125\n\t"
      "v_mfma_f32_16x16x32_bf16 v[132:135], v[164:167], v[116:119], v[132:135]\n\t"
      "ds_read_b64_tr_b16 v[110:111], v15 offset:14048\n\t"
      "v_cvt_pk_bf16_f32 v187, v126, v127\n\t"
      "s_waitcnt lgkmcnt(8)\n\t"
      "v_mfma_f32_16x16x32_bf16 v[120:123], v[136:139], v[88:91], 0\n\t"
      "ds_read_b64_tr_b16 v[112:113], v15 offset:18656\n\t"
      "v_cvt_pk_bf16_f32 v2, v32, v33\n\t"
      "v_mfma_f32_16x16x32_bf16 v[124:127], v[152:155], v[88:91], 0\n\t"
      "ds_read_b64_tr_b16 v[114:115], v15 offset:23264\n\t"
      "v_cvt_pk_bf16_f32 v3, v34, v35\n\t"
      "v_mfma_f32_16x16x32_bf16 v[48:51], v[184:187], v[232:235], v[48:51]\n\t"
      "v_cvt_pk_bf16_f32 v4, v40, v41\n\t"
      "v_cvt_pk_bf16_f32 v5, v42, v43\n\t"
      "v_mfma_f32_16x16x32_bf16 v[52:55], v[184:187], v[236:239], v[52:55]\n\t"
      "buffer_store_dwordx4 v[2:5], v13, s[64:67], 0 offen offset:1024 sc1\n\t"
      "v_add3_u32 v242, v2, v4, v242\n\t"
      "v_mfma_f32_16x16x32_bf16 v[120:123], v[140:143], v[92:95], v[120:123]\n\t"
      "ds_read_b64_tr_b16 v[116:117], v15 offset:27872\n\t"
      "v_add3_u32 v243, v3, v5, v243\n\t"
      "v_mfma_f32_16x16x32_bf16 v[124:127], v[156:159], v[92:95], v[124:127]\n\t"
      "ds_read_b64_tr_b16 v[118:119], v15 offset:32480\n\t"
      "s_waitcnt lgkmcnt(8)\n\t"
      "v_mfma_f32_16x16x32_bf16 v[120:123], v[144:147], v[96:99], v[120:123]\n\t"
      "ds_read_b64_tr_b16 v[88:89], v15 offset:256\n\t"
      "v_cvt_pk_bf16_f32 v188, v128, v129\n\t"
      "v_mfma_f32_16x16x32_bf16 v[124:127], v[160:163], v[96:99], v[124:127]\n\t"
      "ds_read_b64_tr_b16 v[90:91], v15 offset:4864\n\t"
      "v_cvt_pk_bf16_f32 v189, v130, v131\n\t"
      "v_mfma_f32_16x16x32_bf16 v[120:123], v[148:151], v[100:103], v[120:123]\n\t"
      "ds_read_b64_tr_b16 v[92:93], v15 offset:9472\n\t"
      "v_cvt_pk_bf16_f32 v190, v132, v133\n\t"
      "v_mfma_f32_16x16x32_bf16 v[124:127], v[164:167], v[100:103], v[124:127]\n\t"
      "ds_read_b64_tr_b16 v[94:95], v15 offset:14080\n\t"
      "v_cvt_pk_bf16_f32 v191, v134, v135\n\t"
      "s_waitcnt lgkmcnt(8)\n\t"
      "v_mfma_f32_16x16x32_bf16 v[128:131], v[136:139], v[104:107], 0\n\t"
      "ds_read_b64_tr_b16 v[96:97], v15 offset:18688\n\t"
      "v_cvt_pk_bf16_f32 v6, v36, v37\n\t"
      "v_mfma_f32_16x16x32_bf16 v[132:135], v[152:155], v[104:107], 0\n\t"
      "ds_read_b64_tr_b16 v[98:99], v15 offset:23296\n\t"
      "v_cvt_pk_bf16_f32 v7, v38, v39\n\t"
      "v_mfma_f32_16x16x32_bf16 v[56:59], v[188:191], v[232:235], v[56:59]\n\t"
      "v_cvt_pk_bf16_f32 v8, v44, v45\n\t"
      "v_cvt_pk_bf16_f32 v9, v46, v47\n\t"
      "v_mfma_f32_16x16x32_bf16 v[60:63], v[188:191], v[236:239], v[60:63]\n\t"
      "buffer_store_dwordx4 v[6:9], v13, s[68:71], 0 offen offset:1024 sc1\n\t"
      "v_add3_u32 v244, v6, v8, v244\n\t"
      "v_mfma_f32_16x16x32_bf16 v[128:131], v[140:143], v[108:111], v[128:131]\n\t"
      "ds_read_b64_tr_b16 v[100:101], v15 offset:27904\n\t"
      "v_add3_u32 v245, v7, v9, v245\n\t"
      "v_mfma_f32_16x16x32_bf16 v[132:135], v[156:159], v[108:111], v[132:135]\n\t"
      "ds_read_b64_tr_b16 v[102:103], v15 offset:32512\n\t"
      "s_waitcnt lgkmcnt(8)\n\t"
      "v_mfma_f32_16x16x32_bf16 v[128:131], v[144:147], v[112:115], v[128:131]\n\t"
      "v_cvt_pk_bf16_f32 v2, v48, v49\n\t"
      "v_cvt_pk_bf16_f32 v192, v120, v121\n\t"
      "v_mfma_f32_16x16x32_bf16 v[132:135], v[160:163], v[112:115], v[132:135]\n\t"
      "v_cvt_pk_bf16_f32 v3, v50, v51\n\t"
      "v_cvt_pk_bf16_f32 v193, v122, v123\n\t"
      "v_mfma_f32_16x16x32_bf16 v[128:131], v[148:151], v[116:119], v[128:131]\n\t"
      "v_cvt_pk_bf16_f32 v4, v56, v57\n\t"
      "v_cvt_pk_bf16_f32 v194, v124, v125\n\t"
      "v_mfma_f32_16x16x32_bf16 v[132:135], v[164:167], v[116:119], v[132:135]\n\t"
      "v_cvt_pk_bf16_f32 v5, v58, v59\n\t"
      "v_cvt_pk_bf16_f32 v195, v126, v127\n\t"
      "s_waitcnt lgkmcnt(4)\n\t"
      "v_mfma_f32_16x16x32_bf16 v[120:123], v[136:139], v[88:91], 0\n\t"
      "buffer_store_dwordx4 v[2:5], v13, s[64:67], 0 offen offset:2048 sc1\n\t"
      "v_add3_u32 v242, v2, v4, v242\n\t"
      "v_mfma_f32_16x16x32_bf16 v[124:127], v[152:155], v[88:91], 0\n\t"
      "v_add3_u32 v243, v3, v5, v243\n\t"
      "v_cvt_pk_bf16_f32 v6, v52, v53\n\t"
      "v_mfma_f32_16x16x32_bf16 v[64:67], v[192:195], v[232:235], v[64:67]\n\t"
      "v_cvt_pk_bf16_f32 v7, v54, v55\n\t"
      "v_cvt_pk_bf16_f32 v8, v60, v61\n\t"
      "v_mfma_f32_16x16x32_bf16 v[68:71], v[192:195], v[236:239], v[68:71]\n\t"
      "v_cvt_pk_bf16_f32 v9, v62, v63\n\t"
      "buffer_store_dwordx4 v[6:9], v13, s[68:71], 0 offen offset:2048 sc1\n\t"
      "v_mfma_f32_16x16x32_bf16 v[120:123], v[140:143], v[92:95], v[120:123]\n\t"
      "v_add3_u32 v244, v6, v8, v244\n\t"
      "v_add3_u32 v245, v7, v9, v245\n\t"
      "v_mfma_f32_16x16x32_bf16 v[124:127], v[156:159], v[92:95], v[124:127]\n\t"
      "s_waitcnt lgkmcnt(0)\n\t"
      "v_mfma_f32_16x16x32_bf16 v[120:123], v[144:147], v[96:99], v[120:123]\n\t"
      "v_cvt_pk_bf16_f32 v196, v128, v129\n\t"
      "v_mfma_f32_16x16x32_bf16 v[124:127], v[160:163], v[96:99], v[124:127]\n\t"
      "v_cvt_pk_bf16_f32 v197, v130, v131\n\t"
      "v_mfma_f32_16x16x32_bf16 v[120:123], v[148:151], v[100:103], v[120:123]\n\t"
      "v_cvt_pk_bf16_f32 v198, v132, v133\n\t"
      "v_mfma_f32_16x16x32_bf16 v[124:127], v[164:167], v[100:103], v[124:127]\n\t"
      "v_cvt_pk_bf16_f32 v199, v134, v135\n\t"
      "s_nop 1\n\t"
      "v_mfma_f32_16x16x32_bf16 v[72:75], v[196:199], v[232:235], v[72:75]\n\t"
      "v_mfma_f32_16x16x32_bf16 v[76:79], v[196:199], v[236:239], v[76:79]\n\t"
      "s_nop 3\n\t"
      "v_cvt_pk_bf16_f32 v200, v120, v121\n\t"
      "v_cvt_pk_bf16_f32 v201, v122, v123\n\t"
      "v_cvt_pk_bf16_f32 v202, v124, v125\n\t"
      "v_cvt_pk_bf16_f32 v203, v126, v127\n\t"
      "s_nop 1\n\t"
      "v_mfma_f32_16x16x32_bf16 v[80:83], v[200:203], v[232:235], v[80:83]\n\t"
      "v_mfma_f32_16x16x32_bf16 v[84:87], v[200:203], v[236:239], v[84:87]\n\t"
      "v_cvt_pk_bf16_f32 v2, v64, v65\n\t"
      "v_cvt_pk_bf16_f32 v3, v66, v67\n\t"
      "v_cvt_pk_bf16_f32 v4, v72, v73\n\t"
      "v_cvt_pk_bf16_f32 v5, v74, v75\n\t"
      "buffer_store_dwordx4 v[2:5], v13, s[64:67], 0 offen offset:3072 sc1\n\t"
      "v_add3_u32 v242, v2, v4, v242\n\t"
      "v_add3_u32 v243, v3, v5, v243\n\t"
      "v_cvt_pk_bf16_f32 v6, v68, v69\n\t"
      "v_cvt_pk_bf16_f32 v7, v70, v71\n\t"
      "v_cvt_pk_bf16_f32 v8, v76, v77\n\t"
      "v_cvt_pk_bf16_f32 v9, v78, v79\n\t"
      "buffer_store_dwordx4 v[6:9], v13, s[68:71], 0 offen offset:3072 sc1\n\t"
      "v_add3_u32 v244, v6, v8, v244\n\t"
      "v_add3_u32 v245, v7, v9, v245\n\t"
      "v_cvt_pk_bf16_f32 v120, v80, v81\n\t"
      "v_cvt_pk_bf16_f32 v121, v82, v83\n\t"
      "v_add_u32 v122, v242, v120\n\t"
      "v_add_u32 v123, v243, v121\n\t"
      "buffer_store_dwordx4 v[120:123], v13, s[64:67], s56 offen sc1\n\t"
      "v_cvt_pk_bf16_f32 v124, v84, v85\n\t"
      "v_cvt_pk_bf16_f32 v125, v86, v87\n\t"
      "v_add_u32 v126, v244, v124\n\t"
      "v_add_u32 v127, v245, v125\n\t"
      "buffer_store_dwordx4 v[124:127], v13, s[68:71], s56 offen sc1\n\t"
      "s_lshr_b32 s14, s9, 1\n\t"
      "s_cmp_lg_u32 s14, s4\n\t"
      "s_cbranch_scc1 LNOWN%=\n\t"
      "v_add_u32 v10, 109568, v13\n\t"
      "s_bitcmp1_b32 s9, 0\n\t"
      "s_cbranch_scc1 LOWN1%=\n\t"
      "v_cvt_pk_bf16_f32 v88, v16, v17\n\t"
      "v_cvt_pk_bf16_f32 v89, v18, v19\n\t"
      "v_cvt_pk_bf16_f32 v90, v24, v25\n\t"
      "v_cvt_pk_bf16_f32 v91, v26, v27\n\t"
      "ds_write_b128 v10, v[88:91] offset:0\n\t"
      "v_cvt_pk_bf16_f32 v92, v32, v33\n\t"
      "v_cvt_pk_bf16_f32 v93, v34, v35\n\t"
      "v_cvt_pk_bf16_f32 v94, v40, v41\n\t"
      "v_cvt_pk_bf16_f32 v95, v42, v43\n\t"
      "ds_write_b128 v10, v[92:95] offset:1024\n\t"
      "v_cvt_pk_bf16_f32 v96, v48, v49\n\t"
      "v_cvt_pk_bf16_f32 v97, v50, v51\n\t"
      "v_cvt_pk_bf16_f32 v98, v56, v57\n\t"
      "v_cvt_pk_bf16_f32 v99, v58, v59\n\t"
      "ds_write_b128 v10, v[96:99] offset:2048\n\t"
      "v_cvt_pk_bf16_f32 v100, v64, v65\n\t"
      "v_cvt_pk_bf16_f32 v101, v66, v67\n\t"
      "v_cvt_pk_bf16_f32 v102, v72, v73\n\t"
      "v_cvt_pk_bf16_f32 v103, v74, v75\n\t"
      "ds_write_b128 v10, v[100:103] offset:3072\n\t"
      "ds_write_b128 v10, v[120:123] offset:4096\n\t"
      "s_branch LNOWN%=\n\t"
      "LOWN1%=:\n\t"
      "v_cvt_pk_bf16_f32 v88, v20, v21\n\t"
      "v_cvt_pk_bf16_f32 v89, v22, v23\n\t"
      "v_cvt_pk_bf16_f32 v90, v28, v29\n\t"
      "v_cvt_pk_bf16_f32 v91, v30, v31\n\t"
      "ds_write_b128 v10, v[88:91] offset:0\n\t"
      "v_cvt_pk_bf16_f32 v92, v36, v37\n\t"
      "v_cvt_pk_bf16_f32 v93, v38, v39\n\t"
      "v_cvt_pk_bf16_f32 v94, v44, v45\n\t"
      "v_cvt_pk_bf16_f32 v95, v46, v47\n\t"
      "ds_write_b128 v10, v[92:95] offset:1024\n\t"
      "v_cvt_pk_bf16_f32 v96, v52, v53\n\t"
      "v_cvt_pk_bf16_f32 v97, v54, v55\n\t"
      "v_cvt_pk_bf16_f32 v98, v60, v61\n\t"
      "v_cvt_pk_bf16_f32 v99, v62, v63\n\t"
      "ds_write_b128 v10, v[96:99] offset:2048\n\t"
      "v_cvt_pk_bf16_f32 v100, v68, v69\n\t"
      "v_cvt_pk_bf16_f32 v101, v70, v71\n\t"
      "v_cvt_pk_bf16_f32 v102, v76, v77\n\t"
      "v_cvt_pk_bf16_f32 v103, v78, v79\n\t"
      "ds_write_b128 v10, v[100:103] offset:3072\n\t"
      "ds_write_b128 v10, v[124:127] offset:4096\n\t"
      "LNOWN%=:\n\t"
      "LRED%=:\n\t"
      "s_setprio 0\n\t"
      "s_barrier\n\t"
      "s_cmp_lt_u32 s4, 4\n\t"
      "s_cbranch_scc1 LGC%=\n\t"
      "s_add_u32 s14, s9, s4\n\t"
      "s_add_u32 s14, s14, 5\n\t"
      "s_and_b32 s14, s14, 7\n\t"
      "s_lshl3_add_u32 s14, s9, s14\n\t"
      "s_mul_i32 s62, s14, 5248\n\t"
      "s_add_u32 s63, s62, 4096\n\t"
      "s_add_u32 s14, s9, s4\n\t"
      "s_add_u32 s14, s14, 9\n\t"
      "s_and_b32 s14, s14, 7\n\t"
      "s_lshl3_add_u32 s14, s9, s14\n\t"
      "s_mul_i32 s64, s14, 5248\n\t"
      "s_add_u32 s65, s64, 4096\n\t"
      "s_cmp_eq_u32 s4, 7\n\t"
      "s_cselect_b32 s64, s62, s64\n\t"
      "s_cselect_b32 s65, s63, s65\n\t"
      "s_mul_i32 s15, s4, 5120\n\t"
      "s_add_u32 s15, s15, 53248\n\t"
      "v_add_u32 v3, s15, v13\n\t"
      "s_sleep 20\n\t"
      "s_mov_b32 s61, 0\n\t"
      "LGA%=:\n\t"
      "buffer_load_dwordx4 v[16:19], v13, s[36:39], s62 offen offset:0 sc1\n\t"
      "buffer_load_dwordx4 v[20:23], v13, s[36:39], s62 offen offset:1024 sc1\n\t"
      "buffer_load_dwordx4 v[24:27], v13, s[36:39], s62 offen offset:2048 sc1\n\t"
      "buffer_load_dwordx4 v[28:31], v13, s[36:39], s62 offen offset:3072 sc1\n\t"
      "buffer_load_dwordx4 v[32:35], v13, s[36:39], s63 offen sc1\n\t"
      "buffer_load_dwordx4 v[36:39], v13, s[36:39], s64 offen offset:0 sc1\n\t"
      "buffer_load_dwordx4 v[40:43], v13, s[36:39], s64 offen offset:1024 sc1\n\t"
      "buffer_load_dwordx4 v[44:47], v13, s[36:39], s64 offen offset:2048 sc1\n\t"
      "buffer_load_dwordx4 v[48:51], v13, s[36:39], s64 offen offset:3072 sc1\n\t"
      "buffer_load_dwordx4 v[52:55], v13, s[36:39], s65 offen sc1\n\t"
      "s_waitcnt vmcnt(5)\n\t"
      "v_add3_u32 v4, v16, v18, v20\n\t"
      "v_add3_u32 v4, v4, v22, v24\n\t"
      "v_add3_u32 v4, v4, v26, v28\n\t"
      "v_add3_u32 v4, v4, v30, v32\n\t"
      "v_add_u32 v4, s40, v4\n\t"
      "v_xor_b32 v4, v34, v4\n\t"
      "v_add3_u32 v5, v17, v19, v21\n\t"
      "v_add3_u32 v5, v5, v23, v25\n\t"
      "v_add3_u32 v5, v5, v27, v29\n\t"
      "v_add3_u32 v5, v5, v31, v33\n\t"
      "v_add_u32 v5, s41, v5\n\t"
      "v_xor_b32 v5, v35, v5\n\t"
      "s_waitcnt vmcnt(0)\n\t"
      "v_add3_u32 v6, v36, v38, v40\n\t"
      "v_add3_u32 v6, v6, v42, v44\n\t"
      "v_add3_u32 v6, v6, v46, v48\n\t"
      "v_add3_u32 v6, v6, v50, v52\n\t"
      "v_add_u32 v6, s40, v6\n\t"
      "v_xor_b32 v6, v54, v6\n\t"
      "v_add3_u32 v7, v37, v39, v41\n\t"
      "v_add3_u32 v7, v7, v43, v45\n\t"
      "v_add3_u32 v7, v7, v47, v49\n\t"
      "v_add3_u32 v7, v7, v51, v53\n\t"
      "v_add_u32 v7, s41, v7\n\t"
      "v_xor_b32 v7, v55, v7\n\t"
      "v_or3_b32 v4, v4, v5, v6\n\t"
      "v_or_b32 v4, v4, v7\n\t"
      "v_cmp_ne_u32 vcc, 0, v4\n\t"
      "s_cmp_eq_u64 vcc, 0\n\t"
      "s_cbranch_scc1 LGB%=\n\t"
      "s_add_u32 s61, s61, 1\n\t"
      "s_cmp_lt_u32 s61, 0x40000\n\t"
      "s_cbranch_scc1 LGA%=\n\t"
      "LGB%=:\n\t"
      "ds_write_b128 v3, v[16:19] offset:0\n\t"
      "ds_write_b128 v3, v[20:23] offset:1024\n\t"
      "ds_write_b128 v3, v[24:27] offset:2048\n\t"
      "ds_write_b128 v3, v[28:31] offset:3072\n\t"
      "ds_write_b128 v3, v[32:35] offset:4096\n\t"
      "s_cmp_eq_u32 s4, 7\n\t"
      "s_cbranch_scc1 LGC%=\n\t"
      "ds_write_b128 v3, v[36:39] offset:20480\n\t"
      "ds_write_b128 v3, v[40:43] offset:21504\n\t"
      "ds_write_b128 v3, v[44:47] offset:22528\n\t"
      "ds_write_b128 v3, v[48:51] offset:23552\n\t"
      "ds_write_b128 v3, v[52:55] offset:24576\n\t"
      "LGC%=:\n\t"
      "v_and_b32 v2, 15, v1\n\t"
      "v_lshrrev_b32 v3, 4, v1\n\t"
      "v_mul_u32_u24 v2, 2304, v2\n\t"
      "v_lshl_add_u32 v2, v3, 3, v2\n\t"
      "s_lshl3_add_u32 s15, s6, s9\n\t"
      "s_mul_i32 s15, s15, 36864\n\t"
      "s_mul_i32 s16, s5, 1152\n\t"
      "s_add_u32 s15, s15, s16\n\t"
      "s_mul_i32 s16, s11, 288\n\t"
      "s_add_u32 s15, s15, s16\n\t"
      "s_lshl_b32 s16, s4, 6\n\t"
      "s_add_u32 s15, s15, s16\n\t"
      "s_add_u32 s28, s28, s15\n\t"
      "s_addc_u32 s29, s29, 0\n\t"
      "s_lshl_b32 s16, s4, 10\n\t"
      "s_add_u32 s16, s16, 73728\n\t"
      "v_add_u32 v3, s16, v13\n\t"
      "s_waitcnt lgkmcnt(0)\n\t"
      "s_barrier\n\t"
      "s_cmp_gt_u32 s4, 4\n\t"
      "s_cbranch_scc1 LEP%=\n\t"
      "LDRY%=:\n\t"
      "ds_read_b128 v[8:11], v3 offset:0\n\t"
      "ds_read_b128 v[12:15], v3 offset:5120\n\t"
      "ds_read_b128 v[16:19], v3 offset:10240\n\t"
      "ds_read_b128 v[20:23], v3 offset:15360\n\t"
      "ds_read_b128 v[24:27], v3 offset:20480\n\t"
      "ds_read_b128 v[28:31], v3 offset:25600\n\t"
      "ds_read_b128 v[32:35], v3 offset:30720\n\t"
      "ds_read_b128 v[36:39], v3 offset:35840\n\t"
      "s_waitcnt lgkmcnt(7)\n\t"
      "v_lshlrev_b32 v40, 16, v8\n\t"
      "v_and_b32 v41, 0xffff0000, v8\n\t"
      "v_lshlrev_b32 v42, 16, v9\n\t"
      "v_and_b32 v43, 0xffff0000, v9\n\t"
      "v_lshlrev_b32 v44, 16, v10\n\t"
      "v_and_b32 v45, 0xffff0000, v10\n\t"
      "v_lshlrev_b32 v46, 16, v11\n\t"
      "v_and_b32 v47, 0xffff0000, v11\n\t"
      "s_waitcnt lgkmcnt(6)\n\t"
      "v_lshlrev_b32 v48, 16, v12\n\t"
      "v_and_b32 v49, 0xffff0000, v12\n\t"
      "v_lshlrev_b32 v50, 16, v13\n\t"
      "v_and_b32 v51, 0xffff0000, v13\n\t"
      "v_lshlrev_b32 v52, 16, v14\n\t"
      "v_and_b32 v53, 0xffff0000, v14\n\t"
      "v_lshlrev_b32 v54, 16, v15\n\t"
      "v_and_b32 v55, 0xffff0000, v15\n\t"
      "v_pk_add_f32 v[40:41], v[40:41], v[48:49]\n\t"
      "v_pk_add_f32 v[42:43], v[42:43], v[50:51]\n\t"
      "v_pk_add_f32 v[44:45], v[44:45], v[52:53]\n\t"
      "v_pk_add_f32 v[46:47], v[46:47], v[54:55]\n\t"
      "s_waitcnt lgkmcnt(5)\n\t"
      "v_lshlrev_b32 v48, 16, v16\n\t"
      "v_and_b32 v49, 0xffff0000, v16\n\t"
      "v_lshlrev_b32 v50, 16, v17\n\t"
      "v_and_b32 v51, 0xffff0000, v17\n\t"
      "v_lshlrev_b32 v52, 16, v18\n\t"
      "v_and_b32 v53, 0xffff0000, v18\n\t"
      "v_lshlrev_b32 v54, 16, v19\n\t"
      "v_and_b32 v55, 0xffff0000, v19\n\t"
      "v_pk_add_f32 v[40:41], v[40:41], v[48:49]\n\t"
      "v_pk_add_f32 v[42:43], v[42:43], v[50:51]\n\t"
      "v_pk_add_f32 v[44:45], v[44:45], v[52:53]\n\t"
      "v_pk_add_f32 v[46:47], v[46:47], v[54:55]\n\t"
      "s_waitcnt lgkmcnt(4)\n\t"
      "v_lshlrev_b32 v48, 16, v20\n\t"
      "v_and_b32 v49, 0xffff0000, v20\n\t"
      "v_lshlrev_b32 v50, 16, v21\n\t"
      "v_and_b32 v51, 0xffff0000, v21\n\t"
      "v_lshlrev_b32 v52, 16, v22\n\t"
      "v_and_b32 v53, 0xffff0000, v22\n\t"
      "v_lshlrev_b32 v54, 16, v23\n\t"
      "v_and_b32 v55, 0xffff0000, v23\n\t"
      "v_pk_add_f32 v[40:41], v[40:41], v[48:49]\n\t"
      "v_pk_add_f32 v[42:43], v[42:43], v[50:51]\n\t"
      "v_pk_add_f32 v[44:45], v[44:45], v[52:53]\n\t"
      "v_pk_add_f32 v[46:47], v[46:47], v[54:55]\n\t"
      "s_waitcnt lgkmcnt(3)\n\t"
      "v_lshlrev_b32 v48, 16, v24\n\t"
      "v_and_b32 v49, 0xffff0000, v24\n\t"
      "v_lshlrev_b32 v50, 16, v25\n\t"
      "v_and_b32 v51, 0xffff0000, v25\n\t"
      "v_lshlrev_b32 v52, 16, v26\n\t"
      "v_and_b32 v53, 0xffff0000, v26\n\t"
      "v_lshlrev_b32 v54, 16, v27\n\t"
      "v_and_b32 v55, 0xffff0000, v27\n\t"
      "v_pk_add_f32 v[40:41], v[40:41], v[48:49]\n\t"
      "v_pk_add_f32 v[42:43], v[42:43], v[50:51]\n\t"
      "v_pk_add_f32 v[44:45], v[44:45], v[52:53]\n\t"
      "v_pk_add_f32 v[46:47], v[46:47], v[54:55]\n\t"
      "s_waitcnt lgkmcnt(2)\n\t"
      "v_lshlrev_b32 v48, 16, v28\n\t"
      "v_and_b32 v49, 0xffff0000, v28\n\t"
      "v_lshlrev_b32 v50, 16, v29\n\t"
      "v_and_b32 v51, 0xffff0000, v29\n\t"
      "v_lshlrev_b32 v52, 16, v30\n\t"
      "v_and_b32 v53, 0xffff0000, v30\n\t"
      "v_lshlrev_b32 v54, 16, v31\n\t"
      "v_and_b32 v55, 0xffff0000, v31\n\t"
      "v_pk_add_f32 v[40:41], v[40:41], v[48:49]\n\t"
      "v_pk_add_f32 v[42:43], v[42:43], v[50:51]\n\t"
      "v_pk_add_f32 v[44:45], v[44:45], v[52:53]\n\t"
      "v_pk_add_f32 v[46:47], v[46:47], v[54:55]\n\t"
      "s_waitcnt lgkmcnt(1)\n\t"
      "v_lshlrev_b32 v48, 16, v32\n\t"
      "v_and_b32 v49, 0xffff0000, v32\n\t"
      "v_lshlrev_b32 v50, 16, v33\n\t"
      "v_and_b32 v51, 0xffff0000, v33\n\t"
      "v_lshlrev_b32 v52, 16, v34\n\t"
      "v_and_b32 v53, 0xffff0000, v34\n\t"
      "v_lshlrev_b32 v54, 16, v35\n\t"
      "v_and_b32 v55, 0xffff0000, v35\n\t"
      "v_pk_add_f32 v[40:41], v[40:41], v[48:49]\n\t"
      "v_pk_add_f32 v[42:43], v[42:43], v[50:51]\n\t"
      "v_pk_add_f32 v[44:45], v[44:45], v[52:53]\n\t"
      "v_pk_add_f32 v[46:47], v[46:47], v[54:55]\n\t"
      "s_waitcnt lgkmcnt(0)\n\t"
      "v_lshlrev_b32 v48, 16, v36\n\t"
      "v_and_b32 v49, 0xffff0000, v36\n\t"
      "v_lshlrev_b32 v50, 16, v37\n\t"
      "v_and_b32 v51, 0xffff0000, v37\n\t"
      "v_lshlrev_b32 v52, 16, v38\n\t"
      "v_and_b32 v53, 0xffff0000, v38\n\t"
      "v_lshlrev_b32 v54, 16, v39\n\t"
      "v_and_b32 v55, 0xffff0000, v39\n\t"
      "v_pk_add_f32 v[40:41], v[40:41], v[48:49]\n\t"
      "v_pk_add_f32 v[42:43], v[42:43], v[50:51]\n\t"
      "v_pk_add_f32 v[44:45], v[44:45], v[52:53]\n\t"
      "v_pk_add_f32 v[46:47], v[46:47], v[54:55]\n\t"
      "v_cvt_pk_bf16_f32 v56, v40, v41\n\t"
      "v_cvt_pk_bf16_f32 v57, v42, v43\n\t"
      "v_cvt_pk_bf16_f32 v58, v44, v45\n\t"
      "v_cvt_pk_bf16_f32 v59, v46, v47\n\t"
      "global_store_dwordx2 v2, v[56:57], s[28:29]\n\t"
      "s_cmp_eq_u32 s4, 4\n\t"
      "s_cbranch_scc1 LFIN%=\n\t"
      "global_store_dwordx2 v2, v[58:59], s[28:29] offset:32\n\t"
      "s_branch LFIN%=\n\t"
      "LPR1%=:\n\t"
      "s_mov_b32 s54, 0\n\t"
      "LPS1%=:\n\t"
      "buffer_load_dwordx4 v[212:215], v13, s[44:47], s61 offen sc1\n\t"
      "buffer_load_dwordx4 v[216:219], v13, s[44:47], s61 offen offset:1024 sc1\n\t"
      "buffer_load_dwordx2 v[220:221], v14, s[44:47], s61 offen offset:2048 sc1\n\t"
      "s_waitcnt vmcnt(0)\n\t"
      "v_add3_u32 v250, v212, v214, v216\n\t"
      "v_add3_u32 v251, v213, v215, v217\n\t"
      "v_add3_u32 v250, v250, v218, s40\n\t"
      "v_add3_u32 v251, v251, v219, s41\n\t"
      "v_xor_b32 v250, v220, v250\n\t"
      "v_bitop3_b32 v250, v251, v221, v250 bitop3:0xbe\n\t"
      "v_cmp_ne_u32 vcc, 0, v250\n\t"
      "s_cmp_eq_u64 vcc, 0\n\t"
      "s_cbranch_scc1 LPO1%=\n\t"
      "s_add_u32 s54, s54, 1\n\t"
      "s_cmp_lt_u32 s54, 0x40000\n\t"
      "s_cbranch_scc1 LPS1%=\n\t"
      "s_branch LPO1%=\n\t"
      "LPR2%=:\n\t"
      "s_mov_b32 s54, 0\n\t"
      "LPS2%=:\n\t"
      "buffer_load_dwordx4 v[222:225], v13, s[44:47], s62 offen sc1\n\t"
      "buffer_load_dwordx4 v[226:229], v13, s[44:47], s62 offen offset:1024 sc1\n\t"
      "buffer_load_dwordx2 v[230:231], v14, s[44:47], s62 offen offset:2048 sc1\n\t"
      "s_waitcnt vmcnt(0)\n\t"
      "v_add3_u32 v250, v222, v224, v226\n\t"
      "v_add3_u32 v251, v223, v225, v227\n\t"
      "v_add3_u32 v250, v250, v228, s40\n\t"
      "v_add3_u32 v251, v251, v229, s41\n\t"
      "v_xor_b32 v250, v230, v250\n\t"
      "v_bitop3_b32 v250, v251, v231, v250 bitop3:0xbe\n\t"
      "v_cmp_ne_u32 vcc, 0, v250\n\t"
      "s_cmp_eq_u64 vcc, 0\n\t"
      "s_cbranch_scc1 LPO2%=\n\t"
      "s_add_u32 s54, s54, 1\n\t"
      "s_cmp_lt_u32 s54, 0x40000\n\t"
      "s_cbranch_scc1 LPS2%=\n\t"
      "s_branch LPO2%=\n\t"
      "LPR3%=:\n\t"
      "s_mov_b32 s54, 0\n\t"
      "LPS3%=:\n\t"
      "buffer_load_dwordx4 v[232:235], v13, s[44:47], s63 offen sc1\n\t"
      "buffer_load_dwordx4 v[236:239], v13, s[44:47], s63 offen offset:1024 sc1\n\t"
      "buffer_load_dwordx2 v[240:241], v14, s[44:47], s63 offen offset:2048 sc1\n\t"
      "s_waitcnt vmcnt(0)\n\t"
      "v_add3_u32 v250, v232, v234, v236\n\t"
      "v_add3_u32 v251, v233, v235, v237\n\t"
      "v_add3_u32 v250, v250, v238, s40\n\t"
      "v_add3_u32 v251, v251, v239, s41\n\t"
      "v_xor_b32 v250, v240, v250\n\t"
      "v_bitop3_b32 v250, v251, v241, v250 bitop3:0xbe\n\t"
      "v_cmp_ne_u32 vcc, 0, v250\n\t"
      "s_cmp_eq_u64 vcc, 0\n\t"
      "s_cbranch_scc1 LPO3%=\n\t"
      "s_add_u32 s54, s54, 1\n\t"
      "s_cmp_lt_u32 s54, 0x40000\n\t"
      "s_cbranch_scc1 LPS3%=\n\t"
      "s_branch LPO3%=\n\t"
      "LEP%=:\n\t"
      "s_cmp_lg_u32 s4, 7\n\t"
      "s_cbranch_scc1 LFIN%=\n\t"
      "s_or_b32 s14, s9, s11\n\t"
      "s_cmp_lg_u32 s14, 0\n\t"
      "s_cbranch_scc1 LFIN%=\n\t"
      "s_lshl2_add_u32 s14, s5, s6\n\t"
      "s_lshl_b32 s14, s14, 7\n\t"
      "v_mov_b32 v2, s14\n\t"
      "s_xor_b32 s40, s40, 0x5bd1e995\n\t"
      "s_xor_b32 s41, s41, 0x9e3779b9\n\t"
      "v_mov_b32 v4, s40\n\t"
      "v_mov_b32 v5, s41\n\t"
      "s_mov_b64 exec, 1\n\t"
      "global_store_dwordx2 v2, v[4:5], s[30:31] sc1\n\t"
      "s_mov_b64 exec, -1\n\t"
      "LFIN%=:\n\t"
      "LEND%=:\n\t"
      :
      : "s"(__builtin_amdgcn_kernarg_segment_ptr()), "s"(blockIdx.x), "v"(threadIdx.x)
      : "v1", "v2", "v3", "v4", "v5", "v6", "v7", "v8", "v9", "v10", "v11", "v12", "v13", "v14", "v15", "v16", "v17", "v18", "v19", "v20", "v21", "v22", "v23", "v24", "v25", "v26", "v27", "v28", "v29", "v30", "v31", "v32", "v33", "v34", "v35", "v36", "v37", "v38", "v39", "v40", "v41", "v42", "v43", "v44", "v45", "v46", "v47", "v48", "v49", "v50", "v51", "v52", "v53", "v54", "v55", "v56", "v57", "v58", "v59", "v60", "v61", "v62", "v63", "v64", "v65", "v66", "v67", "v68", "v69", "v70", "v71", "v72", "v73", "v74", "v75", "v76", "v77", "v78", "v79", "v80", "v81", "v82", "v83", "v84", "v85", "v86", "v87", "v88", "v89", "v90", "v91", "v92", "v93", "v94", "v95", "v96", "v97", "v98", "v99", "v100", "v101", "v102", "v103", "v104", "v105", "v106", "v107", "v108", "v109", "v110", "v111", "v112", "v113", "v114", "v115", "v116", "v117", "v118", "v119", "v120", "v121", "v122", "v123", "v124", "v125", "v126", "v127", "v128", "v129", "v130", "v131", "v132", "v133", "v134", "v135", "v136", "v137", "v138", "v139", "v140", "v141", "v142", "v143", "v144", "v145", "v146", "v147", "v148", "v149", "v150", "v151", "v152", "v153", "v154", "v155", "v156", "v157", "v158", "v159", "v160", "v161", "v162", "v163", "v164", "v165", "v166", "v167", "v168", "v169", "v170", "v171", "v172", "v173", "v174", "v175", "v176", "v177", "v178", "v179", "v180", "v181", "v182", "v183", "v184", "v185", "v186", "v187", "v188", "v189", "v190", "v191", "v192", "v193", "v194", "v195", "v196", "v197", "v198", "v199", "v200", "v201", "v202", "v203", "v204", "v205", "v206", "v207", "v208", "v209", "v210", "v211", "v212", "v213", "v214", "v215", "v216", "v217", "v218", "v219", "v220", "v221", "v222", "v223", "v224", "v225", "v226", "v227", "v228", "v229", "v230", "v231", "v232", "v233", "v234", "v235", "v236", "v237", "v238", "v239", "v240", "v241", "v242", "v243", "v244", "v245", "v246", "v247", "v248", "v249", "v250", "v251", "v252", "v253", "v254", "v255", "s4", "s5", "s6", "s7", "s8", "s9", "s10", "s11", "s12", "s13", "s14", "s15", "s16", "s17", "s18", "s19", "s20", "s21", "s22", "s23", "s24", "s25", "s26", "s27", "s28", "s29", "s30", "s31", "s33", "s34", "s35", "s36", "s37", "s38", "s39", "s40", "s41", "s42", "s43", "s44", "s45", "s46", "s47", "s48", "s49", "s50", "s51", "s52", "s53", "s54", "s55", "s56", "s57", "s58", "s59", "s60", "s61", "s62", "s63", "s64", "s65", "s66", "s67", "s68", "s69", "s70", "s71", "vcc", "scc", "memory");
}

extern "C" void kernel_launch(void* const* d_in, const int* in_sizes, int n_in, void* d_out, int out_size, void* d_ws, size_t ws_size, hipStream_t stream) {
  (void)hipFuncSetAttribute((const void*)fused_attn, hipFuncAttributeMaxDynamicSharedMemorySize, LDS_BYTES);
  fused_attn<<<256, 512, LDS_BYTES, stream>>>(d_in[0], d_in[1], d_in[2], d_in[3], d_in[4], d_in[5], d_in[6], d_in[7], d_in[8], d_in[9], d_out, d_ws);
}
```
